```python
import torch

def _softmax(x, axis=-1):
    return torch.softmax(x, dim=axis)
def _gelu(x):
    return torch.nn.functional.gelu(x, approximate="tanh")
def _silu(x):
    return torch.nn.functional.silu(x)
def _sigmoid(x):
    return torch.sigmoid(x)
def _layernorm(x, axis=-1, eps=1e-5):
    m = x.mean(dim=axis, keepdim=True)
    v = x.var(dim=axis, keepdim=True, unbiased=False)
    return (x - m) / torch.sqrt(v + eps)
def _scatter_add(tgt, idx, src, axis=0):
    return tgt.clone().index_add_(axis, idx, src)
def _segment_sum(src, seg, n):
    out = torch.zeros((n,) + src.shape[1:], dtype=src.dtype)
    return out.index_add_(0, seg, src)
def _pad(x, pw):
    flat = tuple(v for pair in reversed(pw) for v in pair)
    return torch.nn.functional.pad(x, flat)

kernel_name = 'standalone__glu_gated_swiglu__sampling'

def setup_inputs():

    _w = (lambda a: (lambda t: t.to(torch.bfloat16) if t.is_floating_point() else t)(torch.as_tensor(a)))
    B, S, D, F = 16, 32, 1024, 3584
    return {
        "x": _w((lambda *s: torch.randn(*s, dtype=torch.bfloat16) * 0.05)(B, S, D)),
        "w1": _w((lambda *s: torch.randn(*s, dtype=torch.bfloat16) * 0.05)(D, F) * (2.0 / D) ** 0.5),
        "w3": _w((lambda *s: torch.randn(*s, dtype=torch.bfloat16) * 0.05)(D, F) * (2.0 / D) ** 0.5),
        "w2": _w((lambda *s: torch.randn(*s, dtype=torch.bfloat16) * 0.05)(F, D) * (2.0 / F) ** 0.5),
    }

def reference(x, w1, w3, w2):
    gate = torch.einsum("bsd,df->bsf", x, w1)
    up = torch.einsum("bsd,df->bsf", x, w3)
    h = _silu(gate) * up
    out = torch.einsum("bsf,fd->bsd", h, w2)
    return out
```

```cpp
#include <hip/hip_runtime.h>
#include <hip/hip_bf16.h>
constexpr int M = 512, D = 1024, F = 3584;
__device__ __forceinline__ float bf2f(unsigned short b) { return __uint_as_float((unsigned)b << 16); }
__global__ void k1(const unsigned short* x, const unsigned short* w1, const unsigned short* w3, __hip_bfloat16* h) {
  int f = blockIdx.x * 256 + threadIdx.x, m = blockIdx.y;
  float g = 0, u = 0;
  for (int k = 0; k < D; ++k) { float xv = bf2f(x[m * D + k]); g += xv * bf2f(w1[k * F + f]); u += xv * bf2f(w3[k * F + f]); }
  float s = g / (1.f + __expf(-g));
  h[m * F + f] = __float2bfloat16(s * u);
}
__global__ void k2(const unsigned short* h, const unsigned short* w2, __hip_bfloat16* out) {
  int n = blockIdx.x * 256 + threadIdx.x, m = blockIdx.y;
  float a = 0;
  for (int k = 0; k < F; ++k) a += bf2f(h[m * F + k]) * bf2f(w2[k * D + n]);
  out[m * D + n] = __float2bfloat16(a);
}
extern "C" void kernel_launch(void* const* d_in, const int* in_sizes, int n_in, void* d_out, int out_size, void* d_ws, size_t ws_size, hipStream_t stream) {
  const unsigned short* x = (const unsigned short*)d_in[0];
  const unsigned short* w1 = (const unsigned short*)d_in[1];
  const unsigned short* w3 = (const unsigned short*)d_in[2];
  const unsigned short* w2 = (const unsigned short*)d_in[3];
  k1<<<dim3(F / 256, M), 256, 0, stream>>>(x, w1, w3, (__hip_bfloat16*)d_ws);
  k2<<<dim3(D / 256, M), 256, 0, stream>>>((const unsigned short*)d_ws, w2, (__hip_bfloat16*)d_out);
}
```

```cpp
#include <hip/hip_runtime.h>
#include <hip/hip_bf16.h>

__global__ __launch_bounds__(512, 2) void swiglu_fused(const void* x, unsigned nx, unsigned fx, const void* w1, unsigned n1,
                                                       unsigned f1, const void* w3, unsigned n3, unsigned f3, const void* w2,
                                                       unsigned n2, unsigned f2, void* ws, unsigned nw, unsigned fw, void* out,
                                                       unsigned no, unsigned fo) {
  __shared__ __attribute__((aligned(1024))) char smem[147584];
  asm volatile(
      ".Lentry_%=:\n"
      "s_load_dwordx16 s[4:19], %0, 0\n"
      "s_load_dwordx8 s[20:27], %0, 64\n"
      "s_lshr_b32 s30, %1, 3\n"
      "s_cmp_lg_u32 s30, 28\n"
      "s_cbranch_scc1 .Lno_cpf_%=\n"
      "s_getpc_b64 s[64:65]\n"
      "s_mov_b32 s66, .Lend_%=-.Lentry_%=\n"
      "s_mov_b32 s67, 0x20000\n"
      "s_and_b32 s64, s64, 0xffffff00\n"
      "v_lshlrev_b32 v252, 7, %2\n"
      "buffer_load_dword v252, v252, s[64:67], 0 offen\n"
      ".Lno_cpf_%=:\n"
      "v_readfirstlane_b32 s28, %2\n"
      "v_and_b32 v1, 63, %2\n"
      "s_and_b32 s29, %1, 7\n"
      "s_lshr_b32 s28, s28, 6\n"
      "s_and_b32 s31, s30, 3\n"
      "s_lshr_b32 s55, s30, 2\n"
      "s_mul_i32 s38, s29, 7\n"
      "s_add_u32 s38, s38, s55\n"
      "s_cmp_lt_u32 s28, 4\n"
      "s_cbranch_scc1 .Lother_%=\n"
      "s_cmp_ge_u32 s30, 28\n"
      "s_cbranch_scc1 .Lother_%=\n"
      "s_setprio 3\n"
      "v_lshrrev_b32 v246, 3, v1\n"
      "v_and_b32 v247, 7, v1\n"
      "v_lshrrev_b32 v248, 4, v1\n"
      "v_xor_b32 v249, v247, v248\n"
      "v_lshlrev_b32 v249, 4, v249\n"
      "v_lshl_or_b32 v240, v246, 11, v249\n"
      "v_xor_b32 v241, 64, v240\n"
      "v_add_u32 v241, 15360, v241\n"
      "v_and_b32 v248, 2, v246\n"
      "v_xor_b32 v249, v247, v248\n"
      "v_mul_u32_u24 v246, 7168, v246\n"
      "v_lshl_or_b32 v242, v249, 4, v246\n"
      "v_xor_b32 v243, 64, v242\n"
      "v_add_u32 v243, 56320, v243\n"
      "s_and_b32 s57, s28, 3\n"
      "s_lshl2_add_u32 s69, s31, s57\n"
      "s_lshl_b32 s69, s69, 16\n"
      "s_mul_i32 s70, s57, 114688\n"
      "s_lshl_b32 s55, s38, 7\n"
      "s_add_u32 s70, s70, s55\n"
      "s_lshl_b32 s71, s57, 12\n"
      "s_add_u32 s72, s71, 81920\n"
      "s_add_u32 s56, s31, s38\n"
      "s_mov_b32 m0, s71\n"
      "s_and_b32 s56, s56, 3\n"
      "s_lshl_b32 s66, s56, 7\n"
      "s_mul_i32 s68, s56, 458752\n"
      "s_add_u32 s66, s66, s69\n"
      "s_add_u32 s68, s68, s70\n"
      "s_add_u32 s57, s66, 512\n"
      "s_add_u32 s58, s68, 1835008\n"
      "s_waitcnt lgkmcnt(0)\n"
      "s_bitcmp0_b32 s4, 9\n"
      "s_cselect_b32 s66, s57, s66\n"
      "buffer_load_dwordx4 v240, s[4:7], s66 offen lds\n"
      "s_cselect_b32 s68, s58, s68\n"
      "s_cselect_b32 s39, 0x44444444, 0\n"
      "buffer_load_dwordx4 v241, s[4:7], s66 offen offset:1024 lds\n"
      "s_add_u32 s67, s66, 30720\n"
      "buffer_load_dwordx4 v240, s[4:7], s67 offen offset:2048 lds\n"
      "s_sub_u32 s12, s12, 2048\n"
      "buffer_load_dwordx4 v241, s[4:7], s67 offen offset:3072 lds\n"
      "s_mov_b32 m0, s72\n"
      "s_subb_u32 s13, s13, 0\n"
      "buffer_load_dwordx4 v242, s[8:11], s68 offen lds\n"
      "buffer_load_dwordx4 v243, s[8:11], s68 offen offset:1024 lds\n"
      "buffer_load_dwordx4 v242, s[12:15], s68 offen offset:2048 lds\n"
      "buffer_load_dwordx4 v243, s[12:15], s68 offen offset:3072 lds\n"
      ".Lsetup_%=:\n"
      "s_lshr_b32 s33, s29, 2\n"
      "s_lshr_b32 s55, s16, 7\n"
      "s_sub_u32 s55, 3, s55\n"
      "s_and_b32 s55, s55, 7\n"
      "s_and_b32 s56, s29, 3\n"
      "s_mul_i32 s56, s56, 28\n"
      "s_add_u32 s56, s56, s30\n"
      "s_and_b32 s34, s56, 7\n"
      "s_lshr_b32 s56, s56, 3\n"
      "s_cmp_ge_u32 s56, s55\n"
      "s_addc_u32 s35, s56, 0\n"
      "s_add_u32 s57, s55, 7\n"
      "s_cmp_ge_u32 s56, s57\n"
      "s_addc_u32 s35, s35, 0\n"
      "s_lshl_b32 s56, s29, 2\n"
      "s_and_b32 s57, s56, 8\n"
      "s_add_u32 s55, s55, s57\n"
      "s_and_b32 s56, s56, 4\n"
      "s_add_u32 s56, s56, s31\n"
      "s_cmp_lt_u32 s30, 28\n"
      "s_cselect_b32 s34, s34, s56\n"
      "s_cselect_b32 s35, s35, s55\n"
      "s_and_b32 s55, s34, 1\n"
      "s_lshl_b32 s55, s55, 2\n"
      "s_xor_b32 s55, s55, s28\n"
      "s_mul_i32 s56, s55, 7\n"
      "s_and_b32 s57, s34, 6\n"
      "s_lshl_b32 s40, s57, 16\n"
      "s_lshl_b32 s48, s57, 10\n"
      "s_add_u32 s57, s57, 1\n"
      "s_cmp_eq_u32 s57, 7\n"
      "s_cselect_b32 s57, 0, s57\n"
      "s_lshl_b32 s41, s57, 16\n"
      "s_lshl_b32 s49, s57, 10\n"
      "s_add_u32 s57, s57, 1\n"
      "s_cmp_eq_u32 s57, 7\n"
      "s_cselect_b32 s57, 0, s57\n"
      "s_lshl_b32 s42, s57, 16\n"
      "s_lshl_b32 s50, s57, 10\n"
      "s_add_u32 s57, s57, 1\n"
      "s_cmp_eq_u32 s57, 7\n"
      "s_cselect_b32 s57, 0, s57\n"
      "s_lshl_b32 s43, s57, 16\n"
      "s_lshl_b32 s51, s57, 10\n"
      "s_add_u32 s57, s57, 1\n"
      "s_cmp_eq_u32 s57, 7\n"
      "s_cselect_b32 s57, 0, s57\n"
      "s_lshl_b32 s44, s57, 16\n"
      "s_lshl_b32 s52, s57, 10\n"
      "s_add_u32 s57, s57, 1\n"
      "s_cmp_eq_u32 s57, 7\n"
      "s_cselect_b32 s57, 0, s57\n"
      "s_lshl_b32 s45, s57, 16\n"
      "s_lshl_b32 s53, s57, 10\n"
      "s_add_u32 s57, s57, 1\n"
      "s_cmp_eq_u32 s57, 7\n"
      "s_cselect_b32 s57, 0, s57\n"
      "s_lshl_b32 s46, s57, 16\n"
      "s_lshl_b32 s54, s57, 10\n"
      "s_lshl3_add_u32 s55, s33, s55\n"
      "s_mul_i32 s55, s55, 458752\n"
      "s_lshl_b32 s58, s35, 7\n"
      "s_add_u32 s55, s55, s58\n"
      "v_lshrrev_b32 v246, 3, v1\n"
      "v_lshrrev_b32 v248, 4, v1\n"
      "v_and_b32 v247, 6, v246\n"
      "v_xor_b32 v247, v247, v1\n"
      "v_and_b32 v247, 7, v247\n"
      "v_lshlrev_b32 v247, 4, v247\n"
      "v_lshl_or_b32 v247, v246, 11, v247\n"
      "v_add_u32 v2, s55, v247\n"
      "v_add_u32 v3, 15360, v2\n"
      "v_add_u32 v4, 30720, v2\n"
      "v_add_u32 v5, 46080, v2\n"
      "s_and_b32 s55, s28, 3\n"
      "s_lshr_b32 s59, s28, 2\n"
      "s_lshl4_add_u32 s47, s59, s55\n"
      "s_lshl_b32 s47, s47, 12\n"
      "s_add_u32 s47, s47, 16384\n"
      "v_lshrrev_b32 v247, 2, v1\n"
      "v_lshlrev_b32 v247, 7, v247\n"
      "v_and_b32 v249, 3, v246\n"
      "v_lshl_or_b32 v247, v249, 5, v247\n"
      "v_and_b32 v249, 3, v1\n"
      "v_lshl_or_b32 v247, v249, 3, v247\n"
      "v_add_u32 v6, s47, v247\n"
      "v_xor_b32 v7, 32, v6\n"
      "v_xor_b32 v8, 64, v6\n"
      "v_xor_b32 v9, 96, v6\n"
      "v_and_b32 v250, 15, v1\n"
      "v_xor_b32 v247, v250, v248\n"
      "v_lshlrev_b32 v247, 4, v247\n"
      "v_lshl_or_b32 v247, v248, 8, v247\n"
      "v_add_u32 v10, s47, v247\n"
      "v_xor_b32 v11, 64, v10\n"
      "v_xor_b32 v12, 128, v10\n"
      "v_xor_b32 v13, 192, v10\n"
      "s_lshl_b32 s55, s55, 2\n"
      "v_add_u32 v251, s55, v248\n"
      "v_xor_b32 v247, v251, v250\n"
      "v_lshlrev_b32 v247, 4, v247\n"
      "v_lshl_or_b32 v247, v250, 8, v247\n"
      "s_add_u32 s55, s55, 147472\n"
      "v_mov_b32 v28, s55\n"
      "s_lshl_b32 s55, s59, 14\n"
      "s_add_u32 s55, s55, 16384\n"
      "v_add_u32 v14, s55, v247\n"
      "v_add_u32 v16, 32768, v14\n"
      "v_add_u32 v15, 65536, v14\n"
      "v_add_u32 v17, 65536, v16\n"
      "s_xor_b32 s55, s33, 1\n"
      "s_lshl1_add_u32 s55, s55, s59\n"
      "s_lshl2_add_u32 s55, s34, s55\n"
      "s_lshl_b32 s55, s55, 15\n"
      "s_add_u32 s55, s55, s58\n"
      "v_lshlrev_b32 v247, 11, v251\n"
      "v_lshl_or_b32 v247, v250, 3, v247\n"
      "v_add_u32 v25, s55, v247\n"
      "s_lshl1_add_u32 s55, s34, s33\n"
      "s_lshl_b32 s55, s55, 18\n"
      "v_add_u32 v247, s56, v1\n"
      "v_lshrrev_b32 v249, 5, v247\n"
      "v_lshlrev_b32 v249, 16, v249\n"
      "v_and_b32 v247, 31, v247\n"
      "v_lshl_or_b32 v247, v247, 8, v249\n"
      "s_add_u32 s57, s55, 57344\n"
      "v_add_u32 v18, s57, v247\n"
      "s_lshl_b32 s56, s56, 10\n"
      "s_add_u32 s55, s55, s56\n"
      "s_lshl_b32 s56, s33, 17\n"
      "s_add_u32 s55, s55, s56\n"
      "v_lshl_add_u32 v19, v1, 4, s55\n"
      "v_xor_b32 v20, 65536, v19\n"
      "v_xor_b32 v21, 131072, v19\n"
      "v_xor_b32 v22, 196608, v19\n"
      "s_lshl4_add_u32 s55, s34, s35\n"
      "s_xor_b32 s57, s33, s35\n"
      "s_and_b32 s57, s57, 1\n"
      "s_lshl1_add_u32 s55, s55, s57\n"
      "s_lshl3_add_u32 s55, s55, s28\n"
      "s_lshl_b32 s55, s55, 10\n"
      "s_add_u32 s55, s55, 4194304\n"
      "v_lshl_add_u32 v24, v1, 4, s55\n"
      "v_xor_b32 v23, 8192, v24\n"
      "v_and_b32 v247, 7, v1\n"
      "v_lshlrev_b32 v247, 2, v247\n"
      "v_add_u32 v26, 147520, v247\n"
      "s_lshl2_add_u32 s55, s28, 147520\n"
      "v_mov_b32 v27, s55\n"
      "v_mov_b32 v29, 1\n"
      "s_mov_b64 s[62:63], 0\n"
      "s_cmp_lt_u32 s28, 4\n"
      "s_cbranch_scc1 .Lcons_%=\n"
      "s_cmp_ge_u32 s30, 28\n"
      "s_cbranch_scc1 .Lload_post_%=\n"
      "s_mul_i32 s58, s31, 0x11111111\n"
      "s_and_b32 s59, s38, 3\n"
      "s_mov_b32 s55, 0x88408000\n"
      "s_mov_b32 s56, 0x21030210\n"
      "s_mov_b32 s57, 0x10111\n"
      "s_bitcmp1_b32 s30, 3\n"
      "s_cselect_b32 s55, 0x84808000, s55\n"
      "s_cselect_b32 s56, 0x20130210, s56\n"
      "s_cselect_b32 s57, 0x10111, s57\n"
      "s_bitcmp1_b32 s30, 2\n"
      "s_cselect_b32 s55, 0x88040800, s55\n"
      "s_cselect_b32 s56, 0x21302010, s56\n"
      "s_cselect_b32 s57, 0x101011, s57\n"
      "s_mul_i32 s57, s57, s59\n"
      "s_add_u32 s56, s56, s57\n"
      "s_add_u32 s56, s56, s58\n"
      "s_and_b32 s56, s56, 0x33333333\n"
      "s_xor_b32 s55, s55, s39\n"
      "s_or_b32 s64, s55, s56\n"
      "s_mov_b32 s55, 0xcccc4448\n"
      "s_mov_b32 s56, 0x32103213\n"
      "s_bitcmp1_b32 s30, 3\n"
      "s_cselect_b32 s55, 0xccc4448c, s55\n"
      "s_cselect_b32 s56, 0x32132130, s56\n"
      "s_bitcmp1_b32 s30, 2\n"
      "s_cselect_b32 s55, 0xccc44c48, s55\n"
      "s_cselect_b32 s56, 0x32132013, s56\n"
      "s_add_u32 s56, s56, s58\n"
      "s_and_b32 s56, s56, 0x33333333\n"
      "s_xor_b32 s55, s55, s39\n"
      "s_or_b32 s65, s55, s56\n"
      "s_mov_b32 s77, 124\n"
      "s_lshr_b64 s[64:65], s[64:65], 4\n"
      "s_and_b32 s55, s64, 15\n"
      "s_lshl_b32 s66, s55, 7\n"
      "s_add_u32 s66, s66, s69\n"
      "s_mul_i32 s75, s55, 458752\n"
      "s_add_u32 s67, s66, 30720\n"
      "s_add_u32 s75, s75, s70\n"
      "s_add_u32 m0, s71, 16384\n"
      "s_mov_b32 s73, 5\n"
      "s_branch .Lload_in_%=\n"
      ".Lload_loop_%=:\n"
      "s_waitcnt vmcnt(8)\n"
      "s_barrier\n"
      ".Lload_in_%=:\n"
      "buffer_load_dwordx4 v240, s[4:7], s66 offen lds\n"
      "s_lshr_b64 s[64:65], s[64:65], 4\n"
      "s_and_b32 s55, s64, 15\n"
      "buffer_load_dwordx4 v241, s[4:7], s66 offen offset:1024 lds\n"
      "s_lshl_b32 s66, s55, 7\n"
      "s_lshr_b32 s77, s77, 1\n"
      "s_bitcmp1_b32 s77, 0\n"
      "s_cselect_b64 vcc, 1, 0\n"
      "buffer_load_dwordx4 v240, s[4:7], s67 offen offset:2048 lds\n"
      "s_add_u32 s66, s66, s69\n"
      "s_mul_i32 s76, s55, 458752\n"
      "buffer_load_dwordx4 v241, s[4:7], s67 offen offset:3072 lds\n"
      "s_add_u32 m0, s71, 49152\n"
      "s_add_u32 s67, s66, 30720\n"
      "buffer_load_dwordx4 v242, s[8:11], s75 offen lds\n"
      "s_add_u32 s76, s76, s70\n"
      "buffer_load_dwordx4 v243, s[8:11], s75 offen offset:1024 lds\n"
      "s_add_u32 s57, s71, 32768\n"
      "buffer_load_dwordx4 v242, s[12:15], s75 offen offset:2048 lds\n"
      "buffer_load_dwordx4 v243, s[12:15], s75 offen offset:3072 lds\n"
      "s_cbranch_vccnz .Lsoft1_%=\n"
      ".Lsoft1_ret_%=:\n"
      "s_mov_b32 m0, s57\n"
      "s_waitcnt vmcnt(8)\n"
      "s_barrier\n"
      "buffer_load_dwordx4 v240, s[4:7], s66 offen lds\n"
      "s_lshr_b64 s[64:65], s[64:65], 4\n"
      "s_and_b32 s55, s64, 15\n"
      "buffer_load_dwordx4 v241, s[4:7], s66 offen offset:1024 lds\n"
      "s_lshl_b32 s66, s55, 7\n"
      "s_lshr_b32 s77, s77, 1\n"
      "s_bitcmp1_b32 s77, 0\n"
      "s_cselect_b64 vcc, 1, 0\n"
      "buffer_load_dwordx4 v240, s[4:7], s67 offen offset:2048 lds\n"
      "s_add_u32 s66, s66, s69\n"
      "s_mul_i32 s68, s55, 458752\n"
      "buffer_load_dwordx4 v241, s[4:7], s67 offen offset:3072 lds\n"
      "s_add_u32 m0, s71, 65536\n"
      "s_add_u32 s67, s66, 30720\n"
      "buffer_load_dwordx4 v242, s[8:11], s76 offen lds\n"
      "s_add_u32 s68, s68, s70\n"
      "buffer_load_dwordx4 v243, s[8:11], s76 offen offset:1024 lds\n"
      "s_add_u32 s57, s71, 0\n"
      "buffer_load_dwordx4 v242, s[12:15], s76 offen offset:2048 lds\n"
      "buffer_load_dwordx4 v243, s[12:15], s76 offen offset:3072 lds\n"
      "s_cbranch_vccnz .Lsoft2_%=\n"
      ".Lsoft2_ret_%=:\n"
      "s_mov_b32 m0, s57\n"
      "s_waitcnt vmcnt(8)\n"
      "s_barrier\n"
      "buffer_load_dwordx4 v240, s[4:7], s66 offen lds\n"
      "s_lshr_b64 s[64:65], s[64:65], 4\n"
      "s_and_b32 s55, s64, 15\n"
      "buffer_load_dwordx4 v241, s[4:7], s66 offen offset:1024 lds\n"
      "s_lshl_b32 s66, s55, 7\n"
      "s_lshr_b32 s77, s77, 1\n"
      "s_bitcmp1_b32 s77, 0\n"
      "s_cselect_b64 vcc, 1, 0\n"
      "buffer_load_dwordx4 v240, s[4:7], s67 offen offset:2048 lds\n"
      "s_add_u32 s66, s66, s69\n"
      "s_mul_i32 s75, s55, 458752\n"
      "buffer_load_dwordx4 v241, s[4:7], s67 offen offset:3072 lds\n"
      "s_add_u32 m0, s71, 81920\n"
      "s_add_u32 s67, s66, 30720\n"
      "buffer_load_dwordx4 v242, s[8:11], s68 offen lds\n"
      "s_add_u32 s75, s75, s70\n"
      "buffer_load_dwordx4 v243, s[8:11], s68 offen offset:1024 lds\n"
      "s_add_u32 s57, s71, 16384\n"
      "buffer_load_dwordx4 v242, s[12:15], s68 offen offset:2048 lds\n"
      "s_sub_u32 s73, s73, 1\n"
      "s_cmp_lg_u32 s73, 0\n"
      "buffer_load_dwordx4 v243, s[12:15], s68 offen offset:3072 lds\n"
      "s_cbranch_vccnz .Lsoft0_%=\n"
      ".Lsoft0_ret_%=:\n"
      "s_mov_b32 m0, s57\n"
      "s_cbranch_scc1 .Lload_loop_%=\n"
      "s_waitcnt vmcnt(8)\n"
      ".Lload_post_%=:\n"
      "s_barrier\n"
      "s_add_u32 m0, s47, 16384\n"
      "s_nop 0\n"
      "buffer_load_dwordx4 v2, s[16:19], s40 offen offset:0 lds\n"
      "buffer_load_dwordx4 v3, s[16:19], s40 offen offset:1024 lds\n"
      "buffer_load_dwordx4 v4, s[16:19], s40 offen offset:2048 lds\n"
      "buffer_load_dwordx4 v5, s[16:19], s40 offen offset:3072 lds\n"
      "s_add_u32 m0, s47, 32768\n"
      "s_nop 0\n"
      "buffer_load_dwordx4 v2, s[16:19], s41 offen offset:0 lds\n"
      "buffer_load_dwordx4 v3, s[16:19], s41 offen offset:1024 lds\n"
      "buffer_load_dwordx4 v4, s[16:19], s41 offen offset:2048 lds\n"
      "buffer_load_dwordx4 v5, s[16:19], s41 offen offset:3072 lds\n"
      "s_cmp_ge_u32 s30, 28\n"
      "s_cbranch_scc1 .Lload_w2_%=\n"
      "s_waitcnt vmcnt(8)\n"
      "s_barrier\n"
      "s_barrier\n"
      ".Lload_w2_%=:\n"
      "s_setprio 3\n"
      "s_add_u32 m0, s47, 49152\n"
      "s_nop 0\n"
      "buffer_load_dwordx4 v2, s[16:19], s42 offen offset:0 lds\n"
      "buffer_load_dwordx4 v3, s[16:19], s42 offen offset:1024 lds\n"
      "buffer_load_dwordx4 v4, s[16:19], s42 offen offset:2048 lds\n"
      "buffer_load_dwordx4 v5, s[16:19], s42 offen offset:3072 lds\n"
      "s_add_u32 m0, s47, 0\n"
      "s_nop 0\n"
      "buffer_load_dwordx4 v2, s[16:19], s43 offen offset:0 lds\n"
      "buffer_load_dwordx4 v3, s[16:19], s43 offen offset:1024 lds\n"
      "buffer_load_dwordx4 v4, s[16:19], s43 offen offset:2048 lds\n"
      "buffer_load_dwordx4 v5, s[16:19], s43 offen offset:3072 lds\n"
      "v_mov_b32 v30, 147456\n"
      "ds_read_b64 v[30:31], v30\n"
      "s_waitcnt lgkmcnt(0)\n"
      "v_readfirstlane_b32 s36, v30\n"
      "v_readfirstlane_b32 s37, v31\n"
      "s_waitcnt vmcnt(12)\n"
      "ds_read_b64_tr_b16 v[96:97], v6 offset:16384\n"
      "ds_read_b64_tr_b16 v[98:99], v6 offset:18432\n"
      "ds_read_b64_tr_b16 v[100:101], v7 offset:16384\n"
      "ds_read_b64_tr_b16 v[102:103], v7 offset:18432\n"
      "ds_read_b64_tr_b16 v[104:105], v8 offset:16384\n"
      "ds_read_b64_tr_b16 v[106:107], v8 offset:18432\n"
      "ds_read_b64_tr_b16 v[108:109], v9 offset:16384\n"
      "ds_read_b64_tr_b16 v[110:111], v9 offset:18432\n"
      "s_waitcnt lgkmcnt(0)\n"
      "s_add_u32 m0, s47, 16384\n"
      "s_nop 0\n"
      "buffer_load_dwordx4 v2, s[16:19], s44 offen offset:0 lds\n"
      "buffer_load_dwordx4 v3, s[16:19], s44 offen offset:1024 lds\n"
      "buffer_load_dwordx4 v4, s[16:19], s44 offen offset:2048 lds\n"
      "buffer_load_dwordx4 v5, s[16:19], s44 offen offset:3072 lds\n"
      "s_waitcnt vmcnt(12)\n"
      "ds_read_b64_tr_b16 v[112:113], v6 offset:32768\n"
      "ds_read_b64_tr_b16 v[114:115], v6 offset:34816\n"
      "ds_read_b64_tr_b16 v[116:117], v7 offset:32768\n"
      "ds_read_b64_tr_b16 v[118:119], v7 offset:34816\n"
      "ds_read_b64_tr_b16 v[120:121], v8 offset:32768\n"
      "ds_read_b64_tr_b16 v[122:123], v8 offset:34816\n"
      "ds_read_b64_tr_b16 v[124:125], v9 offset:32768\n"
      "ds_read_b64_tr_b16 v[126:127], v9 offset:34816\n"
      "s_waitcnt lgkmcnt(0)\n"
      "s_add_u32 m0, s47, 32768\n"
      "s_nop 0\n"
      "buffer_load_dwordx4 v2, s[16:19], s45 offen offset:0 lds\n"
      "buffer_load_dwordx4 v3, s[16:19], s45 offen offset:1024 lds\n"
      "buffer_load_dwordx4 v4, s[16:19], s45 offen offset:2048 lds\n"
      "buffer_load_dwordx4 v5, s[16:19], s45 offen offset:3072 lds\n"
      "s_waitcnt vmcnt(12)\n"
      "ds_read_b64_tr_b16 v[128:129], v6 offset:49152\n"
      "ds_read_b64_tr_b16 v[130:131], v6 offset:51200\n"
      "ds_read_b64_tr_b16 v[132:133], v7 offset:49152\n"
      "ds_read_b64_tr_b16 v[134:135], v7 offset:51200\n"
      "ds_read_b64_tr_b16 v[136:137], v8 offset:49152\n"
      "ds_read_b64_tr_b16 v[138:139], v8 offset:51200\n"
      "ds_read_b64_tr_b16 v[140:141], v9 offset:49152\n"
      "ds_read_b64_tr_b16 v[142:143], v9 offset:51200\n"
      "s_waitcnt lgkmcnt(0)\n"
      "s_add_u32 m0, s47, 49152\n"
      "s_nop 0\n"
      "buffer_load_dwordx4 v2, s[16:19], s46 offen offset:0 lds\n"
      "buffer_load_dwordx4 v3, s[16:19], s46 offen offset:1024 lds\n"
      "buffer_load_dwordx4 v4, s[16:19], s46 offen offset:2048 lds\n"
      "buffer_load_dwordx4 v5, s[16:19], s46 offen offset:3072 lds\n"
      "s_waitcnt vmcnt(12)\n"
      "ds_read_b64_tr_b16 v[144:145], v6 offset:0\n"
      "ds_read_b64_tr_b16 v[146:147], v6 offset:2048\n"
      "ds_read_b64_tr_b16 v[148:149], v7 offset:0\n"
      "ds_read_b64_tr_b16 v[150:151], v7 offset:2048\n"
      "ds_read_b64_tr_b16 v[152:153], v8 offset:0\n"
      "ds_read_b64_tr_b16 v[154:155], v8 offset:2048\n"
      "ds_read_b64_tr_b16 v[156:157], v9 offset:0\n"
      "ds_read_b64_tr_b16 v[158:159], v9 offset:2048\n"
      "s_waitcnt vmcnt(8)\n"
      "ds_read_b64_tr_b16 v[160:161], v6 offset:16384\n"
      "ds_read_b64_tr_b16 v[162:163], v6 offset:18432\n"
      "ds_read_b64_tr_b16 v[164:165], v7 offset:16384\n"
      "ds_read_b64_tr_b16 v[166:167], v7 offset:18432\n"
      "ds_read_b64_tr_b16 v[168:169], v8 offset:16384\n"
      "ds_read_b64_tr_b16 v[170:171], v8 offset:18432\n"
      "ds_read_b64_tr_b16 v[172:173], v9 offset:16384\n"
      "ds_read_b64_tr_b16 v[174:175], v9 offset:18432\n"
      "s_waitcnt vmcnt(4)\n"
      "ds_read_b64_tr_b16 v[176:177], v6 offset:32768\n"
      "ds_read_b64_tr_b16 v[178:179], v6 offset:34816\n"
      "ds_read_b64_tr_b16 v[180:181], v7 offset:32768\n"
      "ds_read_b64_tr_b16 v[182:183], v7 offset:34816\n"
      "ds_read_b64_tr_b16 v[184:185], v8 offset:32768\n"
      "ds_read_b64_tr_b16 v[186:187], v8 offset:34816\n"
      "ds_read_b64_tr_b16 v[188:189], v9 offset:32768\n"
      "ds_read_b64_tr_b16 v[190:191], v9 offset:34816\n"
      "s_waitcnt vmcnt(0)\n"
      "ds_read_b64_tr_b16 v[192:193], v6 offset:49152\n"
      "ds_read_b64_tr_b16 v[194:195], v6 offset:51200\n"
      "ds_read_b64_tr_b16 v[196:197], v7 offset:49152\n"
      "ds_read_b64_tr_b16 v[198:199], v7 offset:51200\n"
      "ds_read_b64_tr_b16 v[200:201], v8 offset:49152\n"
      "ds_read_b64_tr_b16 v[202:203], v8 offset:51200\n"
      "ds_read_b64_tr_b16 v[204:205], v9 offset:49152\n"
      "ds_read_b64_tr_b16 v[206:207], v9 offset:51200\n"
      "s_cmp_ge_u32 s30, 28\n"
      "s_cbranch_scc1 .Lpoll_%=\n"
      "ds_read_b32 v30, v28\n"
      "s_waitcnt lgkmcnt(0)\n"
      "v_cmp_ne_u32 vcc, 0, v30\n"
      "s_cbranch_vccz .Lpub_wait_%=\n"
      ".Lpoll_%=:\n"
      "v_mov_b64 v[236:237], 0\n"
      "s_mov_b64 exec, 0x7f\n"
      "buffer_atomic_add_x2 v[236:237], v18, s[20:23], 0 offen sc0 sc1\n"
      "s_mov_b64 exec, -1\n"
      "s_branch .Lp2_%=\n"
      ".Lpub_wait_%=:\n"
      "s_mov_b32 s73, 4194304\n"
      ".Lpub_spin_%=:\n"
      "s_sleep 1\n"
      "ds_read_b32 v30, v28\n"
      "s_sub_u32 s73, s73, 1\n"
      "s_cbranch_scc1 .Lpoll_%=\n"
      "s_waitcnt lgkmcnt(0)\n"
      "v_cmp_ne_u32 vcc, 0, v30\n"
      "s_cbranch_vccz .Lpub_spin_%=\n"
      "s_sleep 8\n"
      "s_branch .Lpoll_%=\n"
      ".Lsoft0_%=:\n"
      "s_waitcnt vmcnt(8)\n"
      "s_nop 15\n"
      "s_nop 3\n"
      "s_branch .Lsoft0_ret_%=\n"
      ".Lsoft1_%=:\n"
      "s_waitcnt vmcnt(8)\n"
      "s_nop 15\n"
      "s_nop 3\n"
      "s_branch .Lsoft1_ret_%=\n"
      ".Lsoft2_%=:\n"
      "s_waitcnt vmcnt(8)\n"
      "s_nop 15\n"
      "s_nop 3\n"
      "s_branch .Lsoft2_ret_%=\n"
      ".Lother_%=:\n"
      "s_waitcnt lgkmcnt(0)\n"
      "s_cmp_lg_u32 s28, 0\n"
      "s_cbranch_scc1 .Lno_epoch_%=\n"
      "v_mov_b32 v30, 6324224\n"
      "buffer_load_dwordx2 v[30:31], v30, s[20:23], 0 offen sc1\n"
      ".Lno_epoch_%=:\n"
      "s_cmp_lt_u32 s30, 28\n"
      "s_cbranch_scc1 .Lsetup_%=\n"
      "s_cmp_ge_u32 s28, 4\n"
      "s_cbranch_scc1 .Lsetup_%=\n"
      "s_bitcmp0_b32 s4, 9\n"
      "s_cselect_b32 s39, 0x44444444, 0\n"
      "s_and_b32 s55, s28, 1\n"
      "s_lshl1_add_u32 s56, s31, s55\n"
      "s_lshl_b32 s56, s56, 17\n"
      "v_lshl_add_u32 v240, v1, 11, s56\n"
      "s_lshl_b32 m0, s28, 8\n"
      "s_cmp_eq_u32 s55, 0\n"
      "s_cselect_b64 s[64:65], s[8:9], s[12:13]\n"
      "s_mov_b64 s[66:67], s[10:11]\n"
      "s_lshr_b32 s56, s64, 7\n"
      "s_mul_i32 s57, s29, 7\n"
      "s_add_u32 s56, s56, s57\n"
      "s_sub_u32 s56, 3, s56\n"
      "s_and_b32 s56, s56, 7\n"
      "s_add_u32 s57, s57, s56\n"
      "s_lshl_b32 s57, s57, 7\n"
      "s_mul_i32 s55, s31, 458752\n"
      "s_add_u32 s57, s57, s55\n"
      "v_mul_u32_u24 v241, 7168, v1\n"
      "v_add_u32 v241, s57, v241\n"
      "s_mov_b32 s58, 0xcc48c4\n"
      "s_mov_b32 s59, 0x312300\n"
      "s_cmp_lt_u32 s28, 2\n"
      "s_cselect_b32 s56, 7, s56\n"
      "s_cselect_b32 s58, 0xc44888, s58\n"
      "s_cselect_b32 s59, 0x231210, s59\n"
      "s_mul_i32 s55, s31, 0x11111111\n"
      "s_add_u32 s59, s59, s55\n"
      "s_and_b32 s59, s59, 0x33333333\n"
      "s_xor_b32 s58, s58, s39\n"
      "s_or_b32 s58, s58, s59\n"
      "s_and_b32 s39, s39, 4\n"
      "s_sleep 24\n"
      "s_cmp_eq_u32 s56, 7\n"
      "s_cbranch_scc1 .Lnoc1_%=\n"
      "s_xor_b32 s55, s39, 8\n"
      "s_mul_i32 s55, s55, 458752\n"
      "buffer_load_dword v241, s[64:67], s55 offen lds\n"
      ".Lnoc1_%=:\n"
      "s_bfe_u32 s55, s58, 0x40000\n"
      "s_lshl_b32 s55, s55, 7\n"
      "buffer_load_dword v240, s[4:7], s55 offen lds\n"
      "s_cmp_eq_u32 s56, 7\n"
      "s_cbranch_scc1 .Lnoc2_%=\n"
      "s_xor_b32 s55, s39, 4\n"
      "s_mul_i32 s55, s55, 458752\n"
      "buffer_load_dword v241, s[64:67], s55 offen lds\n"
      ".Lnoc2_%=:\n"
      "s_bfe_u32 s55, s58, 0x40004\n"
      "s_lshl_b32 s55, s55, 7\n"
      "buffer_load_dword v240, s[4:7], s55 offen lds\n"
      "s_cmp_eq_u32 s56, 7\n"
      "s_cbranch_scc1 .Lnoc3_%=\n"
      "s_xor_b32 s55, s39, 12\n"
      "s_mul_i32 s55, s55, 458752\n"
      "buffer_load_dword v241, s[64:67], s55 offen lds\n"
      ".Lnoc3_%=:\n"
      "s_bfe_u32 s55, s58, 0x40008\n"
      "s_lshl_b32 s55, s55, 7\n"
      "buffer_load_dword v240, s[4:7], s55 offen lds\n"
      "s_bfe_u32 s55, s58, 0x4000c\n"
      "s_lshl_b32 s55, s55, 7\n"
      "buffer_load_dword v240, s[4:7], s55 offen lds\n"
      "s_bfe_u32 s55, s58, 0x40010\n"
      "s_lshl_b32 s55, s55, 7\n"
      "buffer_load_dword v240, s[4:7], s55 offen lds\n"
      "s_bfe_u32 s55, s58, 0x40014\n"
      "s_lshl_b32 s55, s55, 7\n"
      "buffer_load_dword v240, s[4:7], s55 offen lds\n"
      "s_branch .Lsetup_%=\n"
      ".Lcons_%=:\n"
      "s_cmp_lg_u32 s28, 0\n"
      "s_cbranch_scc1 .Lno_tag_%=\n"
      "s_cmp_ge_u32 s30, 28\n"
      "s_cbranch_scc1 .Ltag_%=\n"
      "s_waitcnt vmcnt(0)\n"
      ".Ltag_%=:\n"
      "s_waitcnt vmcnt(6)\n"
      "v_readfirstlane_b32 s36, v30\n"
      "v_readfirstlane_b32 s37, v31\n"
      "v_lshlrev_b32 v246, 2, v1\n"
      "v_add_u32 v246, 147456, v246\n"
      "v_mov_b32 v247, 0\n"
      "s_add_u32 s36, s36, 2135587861\n"
      "s_addc_u32 s37, s37, 2654435769\n"
      "s_lshl_b32 s55, %1, 7\n"
      "s_add_u32 s55, s55, 6291456\n"
      "v_lshl_add_u32 v248, v1, 3, s55\n"
      "v_mov_b32 v30, s36\n"
      "v_mov_b32 v31, s37\n"
      "s_mov_b32 exec_hi, 0\n"
      "ds_write_b32 v246, v247\n"
      "s_mov_b64 exec, 1\n"
      "ds_write_b64 v246, v[30:31]\n"
      "s_mov_b64 exec, 0xffff\n"
      "buffer_store_dwordx2 v[30:31], v248, s[20:23], 0 offen sc1\n"
      "s_mov_b64 exec, -1\n"
      ".Lno_tag_%=:\n"
      "s_cmp_ge_u32 s30, 28\n"
      "s_cbranch_scc1 .Lhelp_%=\n"
      "s_and_b32 s55, s28, 1\n"
      "s_lshr_b32 s56, s28, 1\n"
      "v_lshrrev_b32 v246, 3, v1\n"
      "v_and_b32 v246, 3, v246\n"
      "s_lshl_b32 s57, s55, 1\n"
      "v_xor_b32 v246, s57, v246\n"
      "v_lshlrev_b32 v246, 5, v246\n"
      "v_lshrrev_b32 v247, 5, v1\n"
      "v_lshl_or_b32 v246, v247, 12, v246\n"
      "v_bfe_u32 v247, v1, 4, 1\n"
      "v_lshl_or_b32 v246, v247, 10, v246\n"
      "v_bfe_u32 v247, v1, 2, 2\n"
      "v_lshl_or_b32 v246, v247, 7, v246\n"
      "v_and_b32 v247, 3, v1\n"
      "v_lshl_or_b32 v246, v247, 3, v246\n"
      "v_add_u32 v240, 49152, v246\n"
      "v_xor_b32 v241, 32, v240\n"
      "v_lshrrev_b32 v246, 4, v1\n"
      "v_bfe_u32 v247, v1, 1, 3\n"
      "v_xor_b32 v246, v246, v247\n"
      "v_lshlrev_b32 v246, 4, v246\n"
      "v_and_b32 v247, 15, v1\n"
      "v_lshl_or_b32 v246, v247, 7, v246\n"
      "s_lshl_b32 s57, s56, 13\n"
      "v_add_u32 v242, s57, v246\n"
      "v_xor_b32 v243, 64, v242\n"
      "s_lshl1_add_u32 s56, s31, s56\n"
      "s_lshl1_add_u32 s55, s38, s55\n"
      "s_mul_i32 s57, s33, 56\n"
      "s_sub_u32 s55, s55, s57\n"
      "s_lshl1_add_u32 s57, s56, s33\n"
      "s_lshl_b32 s57, s57, 18\n"
      "s_lshl_b32 s58, s55, 10\n"
      "s_add_u32 s58, s58, s57\n"
      "v_lshl_add_u32 v244, v1, 4, s58\n"
      "s_lshr_b32 s58, s55, 5\n"
      "s_lshl_b32 s58, s58, 16\n"
      "s_add_u32 s57, s57, s58\n"
      "s_and_b32 s55, s55, 31\n"
      "s_lshl_b32 s55, s55, 8\n"
      "s_add_u32 s57, s57, s55\n"
      "s_add_u32 s57, s57, 57344\n"
      "v_lshl_add_u32 v245, v1, 3, s57\n"
      "s_mov_b32 s74, 0xbfb8aa3b\n"
      "s_mov_b32 s75, 0xbfb8aa3b\n"
      "s_mov_b32 s76, 65536\n"
      "s_mov_b32 s77, 131072\n"
      "s_mov_b32 s78, 196608\n"
      "v_mov_b64 v[32:33], 0\n"
      "v_mov_b64 v[34:35], 0\n"
      "v_mov_b64 v[36:37], 0\n"
      "v_mov_b64 v[38:39], 0\n"
      "v_mov_b64 v[40:41], 0\n"
      "v_mov_b64 v[42:43], 0\n"
      "v_mov_b64 v[44:45], 0\n"
      "v_mov_b64 v[46:47], 0\n"
      "v_mov_b64 v[48:49], 0\n"
      "v_mov_b64 v[50:51], 0\n"
      "v_mov_b64 v[52:53], 0\n"
      "v_mov_b64 v[54:55], 0\n"
      "v_mov_b64 v[56:57], 0\n"
      "v_mov_b64 v[58:59], 0\n"
      "v_mov_b64 v[60:61], 0\n"
      "v_mov_b64 v[62:63], 0\n"
      "v_mov_b64 v[64:65], 0\n"
      "v_mov_b64 v[66:67], 0\n"
      "v_mov_b64 v[68:69], 0\n"
      "v_mov_b64 v[70:71], 0\n"
      "v_mov_b64 v[72:73], 0\n"
      "v_mov_b64 v[74:75], 0\n"
      "v_mov_b64 v[76:77], 0\n"
      "v_mov_b64 v[78:79], 0\n"
      "v_mov_b64 v[80:81], 0\n"
      "v_mov_b64 v[82:83], 0\n"
      "v_mov_b64 v[84:85], 0\n"
      "v_mov_b64 v[86:87], 0\n"
      "v_mov_b64 v[88:89], 0\n"
      "v_mov_b64 v[90:91], 0\n"
      "v_mov_b64 v[92:93], 0\n"
      "v_mov_b64 v[94:95], 0\n"
      "v_mov_b64 v[112:113], 0\n"
      "v_mov_b64 v[114:115], 0\n"
      "v_mov_b64 v[116:117], 0\n"
      "v_mov_b64 v[118:119], 0\n"
      "v_mov_b64 v[120:121], 0\n"
      "v_mov_b64 v[122:123], 0\n"
      "v_mov_b64 v[124:125], 0\n"
      "v_mov_b64 v[126:127], 0\n"
      "v_mov_b64 v[192:193], 0\n"
      "v_mov_b64 v[194:195], 0\n"
      "v_mov_b64 v[196:197], 0\n"
      "v_mov_b64 v[198:199], 0\n"
      "v_mov_b64 v[200:201], 0\n"
      "v_mov_b64 v[202:203], 0\n"
      "v_mov_b64 v[204:205], 0\n"
      "v_mov_b64 v[206:207], 0\n"
      "v_mov_b64 v[208:209], 0\n"
      "v_mov_b64 v[210:211], 0\n"
      "v_mov_b64 v[212:213], 0\n"
      "v_mov_b64 v[214:215], 0\n"
      "v_mov_b64 v[216:217], 0\n"
      "v_mov_b64 v[218:219], 0\n"
      "v_mov_b64 v[220:221], 0\n"
      "v_mov_b64 v[222:223], 0\n"
      "s_mov_b32 s73, 5\n"
      ".Lcons_loop_%=:\n"
      "s_waitcnt lgkmcnt(0)\n"
      "s_barrier\n"
      "v_mfma_f32_16x16x32_bf16 v[64:67], v[112:115], v[192:195], v[64:67]\n"
      "ds_read_b64_tr_b16 v[96:97], v240 offset:32768\n"
      "v_mfma_f32_16x16x32_bf16 v[64:67], v[116:119], v[196:199], v[64:67]\n"
      "ds_read_b64_tr_b16 v[98:99], v240 offset:33280\n"
      "v_mfma_f32_16x16x32_bf16 v[68:71], v[112:115], v[200:203], v[68:71]\n"
      "ds_read_b128 v[128:131], v242 offset:0\n"
      "v_mfma_f32_16x16x32_bf16 v[68:71], v[116:119], v[204:207], v[68:71]\n"
      "ds_read_b64_tr_b16 v[100:101], v240 offset:40960\n"
      "v_mfma_f32_16x16x32_bf16 v[72:75], v[112:115], v[208:211], v[72:75]\n"
      "ds_read_b64_tr_b16 v[102:103], v240 offset:41472\n"
      "v_mfma_f32_16x16x32_bf16 v[72:75], v[116:119], v[212:215], v[72:75]\n"
      "ds_read_b128 v[132:135], v243 offset:0\n"
      "v_mfma_f32_16x16x32_bf16 v[76:79], v[112:115], v[216:219], v[76:79]\n"
      "ds_read_b128 v[136:139], v242 offset:2048\n"
      "v_mfma_f32_16x16x32_bf16 v[76:79], v[116:119], v[220:223], v[76:79]\n"
      "ds_read_b128 v[140:143], v243 offset:2048\n"
      "v_mfma_f32_16x16x32_bf16 v[92:95], v[120:123], v[216:219], v[92:95]\n"
      "ds_read_b128 v[144:147], v242 offset:4096\n"
      "v_mfma_f32_16x16x32_bf16 v[92:95], v[124:127], v[220:223], v[92:95]\n"
      "ds_read_b128 v[148:151], v243 offset:4096\n"
      "v_mfma_f32_16x16x32_bf16 v[88:91], v[120:123], v[208:211], v[88:91]\n"
      "ds_read_b128 v[152:155], v242 offset:6144\n"
      "v_mfma_f32_16x16x32_bf16 v[88:91], v[124:127], v[212:215], v[88:91]\n"
      "ds_read_b128 v[156:159], v243 offset:6144\n"
      "v_mfma_f32_16x16x32_bf16 v[84:87], v[120:123], v[200:203], v[84:87]\n"
      "ds_read_b64_tr_b16 v[104:105], v241 offset:32768\n"
      "v_mfma_f32_16x16x32_bf16 v[84:87], v[124:127], v[204:207], v[84:87]\n"
      "ds_read_b64_tr_b16 v[106:107], v241 offset:33280\n"
      "v_mfma_f32_16x16x32_bf16 v[80:83], v[120:123], v[192:195], v[80:83]\n"
      "ds_read_b64_tr_b16 v[108:109], v241 offset:40960\n"
      "v_mfma_f32_16x16x32_bf16 v[80:83], v[124:127], v[196:199], v[80:83]\n"
      "ds_read_b64_tr_b16 v[110:111], v241 offset:41472\n"
      "s_waitcnt lgkmcnt(8)\n"
      "v_mfma_f32_16x16x32_bf16 v[32:35], v[96:99], v[128:131], v[32:35]\n"
      "ds_read_b64_tr_b16 v[112:113], v240 offset:34816\n"
      "v_mfma_f32_16x16x32_bf16 v[32:35], v[100:103], v[132:135], v[32:35]\n"
      "ds_read_b64_tr_b16 v[114:115], v240 offset:35328\n"
      "v_mfma_f32_16x16x32_bf16 v[36:39], v[96:99], v[136:139], v[36:39]\n"
      "ds_read_b64_tr_b16 v[116:117], v240 offset:43008\n"
      "v_mfma_f32_16x16x32_bf16 v[36:39], v[100:103], v[140:143], v[36:39]\n"
      "ds_read_b64_tr_b16 v[118:119], v240 offset:43520\n"
      "s_waitcnt lgkmcnt(8)\n"
      "v_mfma_f32_16x16x32_bf16 v[40:43], v[96:99], v[144:147], v[40:43]\n"
      "ds_read_b64_tr_b16 v[120:121], v241 offset:34816\n"
      "v_mfma_f32_16x16x32_bf16 v[40:43], v[100:103], v[148:151], v[40:43]\n"
      "ds_read_b64_tr_b16 v[122:123], v241 offset:35328\n"
      "v_mfma_f32_16x16x32_bf16 v[44:47], v[96:99], v[152:155], v[44:47]\n"
      "ds_read_b64_tr_b16 v[124:125], v241 offset:43008\n"
      "v_mfma_f32_16x16x32_bf16 v[44:47], v[100:103], v[156:159], v[44:47]\n"
      "ds_read_b64_tr_b16 v[126:127], v241 offset:43520\n"
      "s_waitcnt lgkmcnt(8)\n"
      "v_mfma_f32_16x16x32_bf16 v[60:63], v[104:107], v[152:155], v[60:63]\n"
      "v_mfma_f32_16x16x32_bf16 v[60:63], v[108:111], v[156:159], v[60:63]\n"
      "v_mfma_f32_16x16x32_bf16 v[56:59], v[104:107], v[144:147], v[56:59]\n"
      "v_mfma_f32_16x16x32_bf16 v[56:59], v[108:111], v[148:151], v[56:59]\n"
      "v_mfma_f32_16x16x32_bf16 v[52:55], v[104:107], v[136:139], v[52:55]\n"
      "v_mfma_f32_16x16x32_bf16 v[52:55], v[108:111], v[140:143], v[52:55]\n"
      "v_mfma_f32_16x16x32_bf16 v[48:51], v[104:107], v[128:131], v[48:51]\n"
      "v_mfma_f32_16x16x32_bf16 v[48:51], v[108:111], v[132:135], v[48:51]\n"
      "s_waitcnt lgkmcnt(0)\n"
      "s_barrier\n"
      "v_mfma_f32_16x16x32_bf16 v[64:67], v[112:115], v[128:131], v[64:67]\n"
      "ds_read_b64_tr_b16 v[96:97], v240 offset:0\n"
      "v_mfma_f32_16x16x32_bf16 v[64:67], v[116:119], v[132:135], v[64:67]\n"
      "ds_read_b64_tr_b16 v[98:99], v240 offset:512\n"
      "v_mfma_f32_16x16x32_bf16 v[68:71], v[112:115], v[136:139], v[68:71]\n"
      "ds_read_b128 v[160:163], v242 offset:16384\n"
      "v_mfma_f32_16x16x32_bf16 v[68:71], v[116:119], v[140:143], v[68:71]\n"
      "ds_read_b64_tr_b16 v[100:101], v240 offset:8192\n"
      "v_mfma_f32_16x16x32_bf16 v[72:75], v[112:115], v[144:147], v[72:75]\n"
      "ds_read_b64_tr_b16 v[102:103], v240 offset:8704\n"
      "v_mfma_f32_16x16x32_bf16 v[72:75], v[116:119], v[148:151], v[72:75]\n"
      "ds_read_b128 v[164:167], v243 offset:16384\n"
      "v_mfma_f32_16x16x32_bf16 v[76:79], v[112:115], v[152:155], v[76:79]\n"
      "ds_read_b128 v[168:171], v242 offset:18432\n"
      "v_mfma_f32_16x16x32_bf16 v[76:79], v[116:119], v[156:159], v[76:79]\n"
      "ds_read_b128 v[172:175], v243 offset:18432\n"
      "v_mfma_f32_16x16x32_bf16 v[92:95], v[120:123], v[152:155], v[92:95]\n"
      "ds_read_b128 v[176:179], v242 offset:20480\n"
      "v_mfma_f32_16x16x32_bf16 v[92:95], v[124:127], v[156:159], v[92:95]\n"
      "ds_read_b128 v[180:183], v243 offset:20480\n"
      "v_mfma_f32_16x16x32_bf16 v[88:91], v[120:123], v[144:147], v[88:91]\n"
      "ds_read_b128 v[184:187], v242 offset:22528\n"
      "v_mfma_f32_16x16x32_bf16 v[88:91], v[124:127], v[148:151], v[88:91]\n"
      "ds_read_b128 v[188:191], v243 offset:22528\n"
      "v_mfma_f32_16x16x32_bf16 v[84:87], v[120:123], v[136:139], v[84:87]\n"
      "ds_read_b64_tr_b16 v[104:105], v241 offset:0\n"
      "v_mfma_f32_16x16x32_bf16 v[84:87], v[124:127], v[140:143], v[84:87]\n"
      "ds_read_b64_tr_b16 v[106:107], v241 offset:512\n"
      "v_mfma_f32_16x16x32_bf16 v[80:83], v[120:123], v[128:131], v[80:83]\n"
      "ds_read_b64_tr_b16 v[108:109], v241 offset:8192\n"
      "v_mfma_f32_16x16x32_bf16 v[80:83], v[124:127], v[132:135], v[80:83]\n"
      "ds_read_b64_tr_b16 v[110:111], v241 offset:8704\n"
      "s_waitcnt lgkmcnt(8)\n"
      "v_mfma_f32_16x16x32_bf16 v[32:35], v[96:99], v[160:163], v[32:35]\n"
      "ds_read_b64_tr_b16 v[112:113], v240 offset:2048\n"
      "v_mfma_f32_16x16x32_bf16 v[32:35], v[100:103], v[164:167], v[32:35]\n"
      "ds_read_b64_tr_b16 v[114:115], v240 offset:2560\n"
      "v_mfma_f32_16x16x32_bf16 v[36:39], v[96:99], v[168:171], v[36:39]\n"
      "ds_read_b64_tr_b16 v[116:117], v240 offset:10240\n"
      "v_mfma_f32_16x16x32_bf16 v[36:39], v[100:103], v[172:175], v[36:39]\n"
      "ds_read_b64_tr_b16 v[118:119], v240 offset:10752\n"
      "s_waitcnt lgkmcnt(8)\n"
      "v_mfma_f32_16x16x32_bf16 v[40:43], v[96:99], v[176:179], v[40:43]\n"
      "ds_read_b64_tr_b16 v[120:121], v241 offset:2048\n"
      "v_mfma_f32_16x16x32_bf16 v[40:43], v[100:103], v[180:183], v[40:43]\n"
      "ds_read_b64_tr_b16 v[122:123], v241 offset:2560\n"
      "v_mfma_f32_16x16x32_bf16 v[44:47], v[96:99], v[184:187], v[44:47]\n"
      "ds_read_b64_tr_b16 v[124:125], v241 offset:10240\n"
      "v_mfma_f32_16x16x32_bf16 v[44:47], v[100:103], v[188:191], v[44:47]\n"
      "ds_read_b64_tr_b16 v[126:127], v241 offset:10752\n"
      "s_waitcnt lgkmcnt(8)\n"
      "v_mfma_f32_16x16x32_bf16 v[60:63], v[104:107], v[184:187], v[60:63]\n"
      "v_mfma_f32_16x16x32_bf16 v[60:63], v[108:111], v[188:191], v[60:63]\n"
      "v_mfma_f32_16x16x32_bf16 v[56:59], v[104:107], v[176:179], v[56:59]\n"
      "v_mfma_f32_16x16x32_bf16 v[56:59], v[108:111], v[180:183], v[56:59]\n"
      "v_mfma_f32_16x16x32_bf16 v[52:55], v[104:107], v[168:171], v[52:55]\n"
      "v_mfma_f32_16x16x32_bf16 v[52:55], v[108:111], v[172:175], v[52:55]\n"
      "v_mfma_f32_16x16x32_bf16 v[48:51], v[104:107], v[160:163], v[48:51]\n"
      "v_mfma_f32_16x16x32_bf16 v[48:51], v[108:111], v[164:167], v[48:51]\n"
      "s_waitcnt lgkmcnt(0)\n"
      "s_barrier\n"
      "v_mfma_f32_16x16x32_bf16 v[64:67], v[112:115], v[160:163], v[64:67]\n"
      "ds_read_b64_tr_b16 v[96:97], v240 offset:16384\n"
      "v_mfma_f32_16x16x32_bf16 v[64:67], v[116:119], v[164:167], v[64:67]\n"
      "ds_read_b64_tr_b16 v[98:99], v240 offset:16896\n"
      "v_mfma_f32_16x16x32_bf16 v[68:71], v[112:115], v[168:171], v[68:71]\n"
      "ds_read_b128 v[192:195], v242 offset:32768\n"
      "v_mfma_f32_16x16x32_bf16 v[68:71], v[116:119], v[172:175], v[68:71]\n"
      "ds_read_b64_tr_b16 v[100:101], v240 offset:24576\n"
      "v_mfma_f32_16x16x32_bf16 v[72:75], v[112:115], v[176:179], v[72:75]\n"
      "ds_read_b64_tr_b16 v[102:103], v240 offset:25088\n"
      "v_mfma_f32_16x16x32_bf16 v[72:75], v[116:119], v[180:183], v[72:75]\n"
      "ds_read_b128 v[196:199], v243 offset:32768\n"
      "v_mfma_f32_16x16x32_bf16 v[76:79], v[112:115], v[184:187], v[76:79]\n"
      "ds_read_b128 v[200:203], v242 offset:34816\n"
      "v_mfma_f32_16x16x32_bf16 v[76:79], v[116:119], v[188:191], v[76:79]\n"
      "ds_read_b128 v[204:207], v243 offset:34816\n"
      "v_mfma_f32_16x16x32_bf16 v[92:95], v[120:123], v[184:187], v[92:95]\n"
      "ds_read_b128 v[208:211], v242 offset:36864\n"
      "v_mfma_f32_16x16x32_bf16 v[92:95], v[124:127], v[188:191], v[92:95]\n"
      "ds_read_b128 v[212:215], v243 offset:36864\n"
      "v_mfma_f32_16x16x32_bf16 v[88:91], v[120:123], v[176:179], v[88:91]\n"
      "ds_read_b128 v[216:219], v242 offset:38912\n"
      "v_mfma_f32_16x16x32_bf16 v[88:91], v[124:127], v[180:183], v[88:91]\n"
      "ds_read_b128 v[220:223], v243 offset:38912\n"
      "v_mfma_f32_16x16x32_bf16 v[84:87], v[120:123], v[168:171], v[84:87]\n"
      "ds_read_b64_tr_b16 v[104:105], v241 offset:16384\n"
      "v_mfma_f32_16x16x32_bf16 v[84:87], v[124:127], v[172:175], v[84:87]\n"
      "ds_read_b64_tr_b16 v[106:107], v241 offset:16896\n"
      "v_mfma_f32_16x16x32_bf16 v[80:83], v[120:123], v[160:163], v[80:83]\n"
      "ds_read_b64_tr_b16 v[108:109], v241 offset:24576\n"
      "v_mfma_f32_16x16x32_bf16 v[80:83], v[124:127], v[164:167], v[80:83]\n"
      "ds_read_b64_tr_b16 v[110:111], v241 offset:25088\n"
      "s_sub_u32 s73, s73, 1\n"
      "s_cmp_lg_u32 s73, 0\n"
      "s_waitcnt lgkmcnt(8)\n"
      "v_mfma_f32_16x16x32_bf16 v[32:35], v[96:99], v[192:195], v[32:35]\n"
      "ds_read_b64_tr_b16 v[112:113], v240 offset:18432\n"
      "v_mfma_f32_16x16x32_bf16 v[32:35], v[100:103], v[196:199], v[32:35]\n"
      "ds_read_b64_tr_b16 v[114:115], v240 offset:18944\n"
      "v_mfma_f32_16x16x32_bf16 v[36:39], v[96:99], v[200:203], v[36:39]\n"
      "ds_read_b64_tr_b16 v[116:117], v240 offset:26624\n"
      "v_mfma_f32_16x16x32_bf16 v[36:39], v[100:103], v[204:207], v[36:39]\n"
      "ds_read_b64_tr_b16 v[118:119], v240 offset:27136\n"
      "s_waitcnt lgkmcnt(8)\n"
      "v_mfma_f32_16x16x32_bf16 v[40:43], v[96:99], v[208:211], v[40:43]\n"
      "ds_read_b64_tr_b16 v[120:121], v241 offset:18432\n"
      "v_mfma_f32_16x16x32_bf16 v[40:43], v[100:103], v[212:215], v[40:43]\n"
      "ds_read_b64_tr_b16 v[122:123], v241 offset:18944\n"
      "v_mfma_f32_16x16x32_bf16 v[44:47], v[96:99], v[216:219], v[44:47]\n"
      "ds_read_b64_tr_b16 v[124:125], v241 offset:26624\n"
      "v_mfma_f32_16x16x32_bf16 v[44:47], v[100:103], v[220:223], v[44:47]\n"
      "ds_read_b64_tr_b16 v[126:127], v241 offset:27136\n"
      "s_waitcnt lgkmcnt(8)\n"
      "v_mfma_f32_16x16x32_bf16 v[60:63], v[104:107], v[216:219], v[60:63]\n"
      "v_mfma_f32_16x16x32_bf16 v[60:63], v[108:111], v[220:223], v[60:63]\n"
      "v_mfma_f32_16x16x32_bf16 v[56:59], v[104:107], v[208:211], v[56:59]\n"
      "v_mfma_f32_16x16x32_bf16 v[56:59], v[108:111], v[212:215], v[56:59]\n"
      "v_mfma_f32_16x16x32_bf16 v[52:55], v[104:107], v[200:203], v[52:55]\n"
      "v_mfma_f32_16x16x32_bf16 v[52:55], v[108:111], v[204:207], v[52:55]\n"
      "v_mfma_f32_16x16x32_bf16 v[48:51], v[104:107], v[192:195], v[48:51]\n"
      "v_mfma_f32_16x16x32_bf16 v[48:51], v[108:111], v[196:199], v[48:51]\n"
      "s_cbranch_scc1 .Lcons_loop_%=\n"
      "v_mov_b32 v30, 147456\n"
      "s_waitcnt lgkmcnt(0)\n"
      "s_barrier\n"
      "v_mfma_f32_16x16x32_bf16 v[64:67], v[112:115], v[192:195], v[64:67]\n"
      "ds_read_b64_tr_b16 v[96:97], v240 offset:32768\n"
      "ds_read_b64_tr_b16 v[98:99], v240 offset:33280\n"
      "v_mfma_f32_16x16x32_bf16 v[64:67], v[116:119], v[196:199], v[64:67]\n"
      "ds_read_b128 v[128:131], v242 offset:0\n"
      "ds_read_b64_tr_b16 v[100:101], v240 offset:40960\n"
      "v_mfma_f32_16x16x32_bf16 v[68:71], v[112:115], v[200:203], v[68:71]\n"
      "ds_read_b64_tr_b16 v[102:103], v240 offset:41472\n"
      "ds_read_b128 v[132:135], v243 offset:0\n"
      "v_mfma_f32_16x16x32_bf16 v[68:71], v[116:119], v[204:207], v[68:71]\n"
      "ds_read_b64_tr_b16 v[104:105], v241 offset:32768\n"
      "ds_read_b64_tr_b16 v[106:107], v241 offset:33280\n"
      "v_mfma_f32_16x16x32_bf16 v[72:75], v[112:115], v[208:211], v[72:75]\n"
      "ds_read_b64_tr_b16 v[108:109], v241 offset:40960\n"
      "ds_read_b64_tr_b16 v[110:111], v241 offset:41472\n"
      "v_mfma_f32_16x16x32_bf16 v[72:75], v[116:119], v[212:215], v[72:75]\n"
      "ds_read_b64_tr_b16 v[224:225], v240 offset:34816\n"
      "ds_read_b64_tr_b16 v[226:227], v240 offset:35328\n"
      "v_mfma_f32_16x16x32_bf16 v[76:79], v[112:115], v[216:219], v[76:79]\n"
      "ds_read_b64_tr_b16 v[228:229], v240 offset:43008\n"
      "ds_read_b64_tr_b16 v[230:231], v240 offset:43520\n"
      "v_mfma_f32_16x16x32_bf16 v[76:79], v[116:119], v[220:223], v[76:79]\n"
      "ds_read_b64_tr_b16 v[232:233], v241 offset:34816\n"
      "ds_read_b64_tr_b16 v[234:235], v241 offset:35328\n"
      "v_mfma_f32_16x16x32_bf16 v[92:95], v[120:123], v[216:219], v[92:95]\n"
      "ds_read_b64_tr_b16 v[236:237], v241 offset:43008\n"
      "ds_read_b64_tr_b16 v[238:239], v241 offset:43520\n"
      "v_mfma_f32_16x16x32_bf16 v[92:95], v[124:127], v[220:223], v[92:95]\n"
      "ds_read_b128 v[136:139], v242 offset:2048\n"
      "ds_read_b128 v[140:143], v243 offset:2048\n"
      "v_mfma_f32_16x16x32_bf16 v[88:91], v[120:123], v[208:211], v[88:91]\n"
      "ds_read_b128 v[144:147], v242 offset:4096\n"
      "ds_read_b128 v[148:151], v243 offset:4096\n"
      "v_mfma_f32_16x16x32_bf16 v[88:91], v[124:127], v[212:215], v[88:91]\n"
      "ds_read_b128 v[152:155], v242 offset:6144\n"
      "ds_read_b128 v[156:159], v243 offset:6144\n"
      "v_mfma_f32_16x16x32_bf16 v[84:87], v[120:123], v[200:203], v[84:87]\n"
      "ds_read_b64 v[30:31], v30\n"
      "v_mfma_f32_16x16x32_bf16 v[84:87], v[124:127], v[204:207], v[84:87]\n"
      "v_mfma_f32_16x16x32_bf16 v[80:83], v[120:123], v[192:195], v[80:83]\n"
      "v_mfma_f32_16x16x32_bf16 v[80:83], v[124:127], v[196:199], v[80:83]\n"
      "s_setprio 3\n"
      "s_waitcnt lgkmcnt(15)\n"
      "v_mfma_f32_16x16x32_bf16 v[32:35], v[96:99], v[128:131], v[32:35]\n"
      "v_mfma_f32_16x16x32_bf16 v[32:35], v[100:103], v[132:135], v[32:35]\n"
      "v_mfma_f32_16x16x32_bf16 v[48:51], v[104:107], v[128:131], v[48:51]\n"
      "v_mfma_f32_16x16x32_bf16 v[48:51], v[108:111], v[132:135], v[48:51]\n"
      "s_waitcnt lgkmcnt(13)\n"
      "v_mfma_f32_16x16x32_bf16 v[64:67], v[224:227], v[128:131], v[64:67]\n"
      "s_waitcnt lgkmcnt(11)\n"
      "v_mfma_f32_16x16x32_bf16 v[64:67], v[228:231], v[132:135], v[64:67]\n"
      "s_waitcnt lgkmcnt(9)\n"
      "v_mfma_f32_16x16x32_bf16 v[80:83], v[232:235], v[128:131], v[80:83]\n"
      "s_waitcnt lgkmcnt(0)\n"
      "s_barrier\n"
      "v_mfma_f32_16x16x32_bf16 v[80:83], v[236:239], v[132:135], v[80:83]\n"
      "v_readfirstlane_b32 s36, v30\n"
      "v_readfirstlane_b32 s37, v31\n"
      "v_pk_mul_f32 v[208:209], v[32:33], s[74:75] op_sel_hi:[1,0]\n"
      "v_pk_mul_f32 v[210:211], v[34:35], s[74:75] op_sel_hi:[1,0]\n"
      "v_pk_mul_f32 v[212:213], v[48:49], s[74:75] op_sel_hi:[1,0]\n"
      "v_pk_mul_f32 v[214:215], v[50:51], s[74:75] op_sel_hi:[1,0]\n"
      "v_mfma_f32_16x16x32_bf16 v[36:39], v[96:99], v[136:139], v[36:39]\n"
      "v_exp_f32 v208, v208\n"
      "v_exp_f32 v209, v209\n"
      "v_mfma_f32_16x16x32_bf16 v[36:39], v[100:103], v[140:143], v[36:39]\n"
      "v_exp_f32 v210, v210\n"
      "v_exp_f32 v211, v211\n"
      "v_mfma_f32_16x16x32_bf16 v[52:55], v[104:107], v[136:139], v[52:55]\n"
      "v_exp_f32 v212, v212\n"
      "v_exp_f32 v213, v213\n"
      "v_mfma_f32_16x16x32_bf16 v[52:55], v[108:111], v[140:143], v[52:55]\n"
      "v_exp_f32 v214, v214\n"
      "v_exp_f32 v215, v215\n"
      "v_pk_add_f32 v[208:209], v[208:209], 1.0 op_sel_hi:[1,0]\n"
      "v_pk_add_f32 v[210:211], v[210:211], 1.0 op_sel_hi:[1,0]\n"
      "v_pk_add_f32 v[212:213], v[212:213], 1.0 op_sel_hi:[1,0]\n"
      "v_pk_add_f32 v[214:215], v[214:215], 1.0 op_sel_hi:[1,0]\n"
      "v_mfma_f32_16x16x32_bf16 v[68:71], v[224:227], v[136:139], v[68:71]\n"
      "v_rcp_f32 v208, v208\n"
      "v_rcp_f32 v209, v209\n"
      "v_mfma_f32_16x16x32_bf16 v[68:71], v[228:231], v[140:143], v[68:71]\n"
      "v_rcp_f32 v210, v210\n"
      "v_rcp_f32 v211, v211\n"
      "v_mfma_f32_16x16x32_bf16 v[84:87], v[232:235], v[136:139], v[84:87]\n"
      "v_rcp_f32 v212, v212\n"
      "v_rcp_f32 v213, v213\n"
      "v_mfma_f32_16x16x32_bf16 v[84:87], v[236:239], v[140:143], v[84:87]\n"
      "v_rcp_f32 v214, v214\n"
      "v_rcp_f32 v215, v215\n"
      "v_pk_mul_f32 v[208:209], v[208:209], v[32:33]\n"
      "v_pk_mul_f32 v[210:211], v[210:211], v[34:35]\n"
      "v_pk_mul_f32 v[212:213], v[212:213], v[48:49]\n"
      "v_pk_mul_f32 v[214:215], v[214:215], v[50:51]\n"
      "v_pk_mul_f32 v[208:209], v[208:209], v[64:65]\n"
      "v_pk_mul_f32 v[210:211], v[210:211], v[66:67]\n"
      "v_pk_mul_f32 v[212:213], v[212:213], v[80:81]\n"
      "v_pk_mul_f32 v[214:215], v[214:215], v[82:83]\n"
      "v_cvt_pk_bf16_f32 v192, v208, v209\n"
      "v_cvt_pk_bf16_f32 v193, v210, v211\n"
      "v_cvt_pk_bf16_f32 v194, v212, v213\n"
      "v_cvt_pk_bf16_f32 v195, v214, v215\n"
      "v_pk_mul_f32 v[208:209], v[36:37], s[74:75] op_sel_hi:[1,0]\n"
      "v_pk_mul_f32 v[210:211], v[38:39], s[74:75] op_sel_hi:[1,0]\n"
      "v_pk_mul_f32 v[212:213], v[52:53], s[74:75] op_sel_hi:[1,0]\n"
      "v_pk_mul_f32 v[214:215], v[54:55], s[74:75] op_sel_hi:[1,0]\n"
      "v_mfma_f32_16x16x32_bf16 v[40:43], v[96:99], v[144:147], v[40:43]\n"
      "v_exp_f32 v208, v208\n"
      "v_exp_f32 v209, v209\n"
      "v_mfma_f32_16x16x32_bf16 v[40:43], v[100:103], v[148:151], v[40:43]\n"
      "v_exp_f32 v210, v210\n"
      "v_exp_f32 v211, v211\n"
      "v_mfma_f32_16x16x32_bf16 v[56:59], v[104:107], v[144:147], v[56:59]\n"
      "v_exp_f32 v212, v212\n"
      "v_exp_f32 v213, v213\n"
      "v_mfma_f32_16x16x32_bf16 v[56:59], v[108:111], v[148:151], v[56:59]\n"
      "v_exp_f32 v214, v214\n"
      "v_exp_f32 v215, v215\n"
      "v_pk_add_f32 v[208:209], v[208:209], 1.0 op_sel_hi:[1,0]\n"
      "v_pk_add_f32 v[210:211], v[210:211], 1.0 op_sel_hi:[1,0]\n"
      "v_pk_add_f32 v[212:213], v[212:213], 1.0 op_sel_hi:[1,0]\n"
      "v_pk_add_f32 v[214:215], v[214:215], 1.0 op_sel_hi:[1,0]\n"
      "v_mfma_f32_16x16x32_bf16 v[72:75], v[224:227], v[144:147], v[72:75]\n"
      "v_rcp_f32 v208, v208\n"
      "v_rcp_f32 v209, v209\n"
      "v_mfma_f32_16x16x32_bf16 v[72:75], v[228:231], v[148:151], v[72:75]\n"
      "v_rcp_f32 v210, v210\n"
      "v_rcp_f32 v211, v211\n"
      "v_mfma_f32_16x16x32_bf16 v[88:91], v[232:235], v[144:147], v[88:91]\n"
      "v_rcp_f32 v212, v212\n"
      "v_rcp_f32 v213, v213\n"
      "v_mfma_f32_16x16x32_bf16 v[88:91], v[236:239], v[148:151], v[88:91]\n"
      "v_rcp_f32 v214, v214\n"
      "v_rcp_f32 v215, v215\n"
      "v_pk_mul_f32 v[208:209], v[208:209], v[36:37]\n"
      "v_pk_mul_f32 v[210:211], v[210:211], v[38:39]\n"
      "v_pk_mul_f32 v[212:213], v[212:213], v[52:53]\n"
      "v_pk_mul_f32 v[214:215], v[214:215], v[54:55]\n"
      "v_pk_mul_f32 v[208:209], v[208:209], v[68:69]\n"
      "v_pk_mul_f32 v[210:211], v[210:211], v[70:71]\n"
      "v_pk_mul_f32 v[212:213], v[212:213], v[84:85]\n"
      "v_pk_mul_f32 v[214:215], v[214:215], v[86:87]\n"
      "v_cvt_pk_bf16_f32 v196, v208, v209\n"
      "v_cvt_pk_bf16_f32 v197, v210, v211\n"
      "v_cvt_pk_bf16_f32 v198, v212, v213\n"
      "v_cvt_pk_bf16_f32 v199, v214, v215\n"
      "v_pk_mul_f32 v[208:209], v[40:41], s[74:75] op_sel_hi:[1,0]\n"
      "v_pk_mul_f32 v[210:211], v[42:43], s[74:75] op_sel_hi:[1,0]\n"
      "v_pk_mul_f32 v[212:213], v[56:57], s[74:75] op_sel_hi:[1,0]\n"
      "v_pk_mul_f32 v[214:215], v[58:59], s[74:75] op_sel_hi:[1,0]\n"
      "v_mfma_f32_16x16x32_bf16 v[44:47], v[96:99], v[152:155], v[44:47]\n"
      "v_exp_f32 v208, v208\n"
      "v_exp_f32 v209, v209\n"
      "v_mfma_f32_16x16x32_bf16 v[44:47], v[100:103], v[156:159], v[44:47]\n"
      "v_exp_f32 v210, v210\n"
      "v_exp_f32 v211, v211\n"
      "v_mfma_f32_16x16x32_bf16 v[60:63], v[104:107], v[152:155], v[60:63]\n"
      "v_exp_f32 v212, v212\n"
      "v_exp_f32 v213, v213\n"
      "v_mfma_f32_16x16x32_bf16 v[60:63], v[108:111], v[156:159], v[60:63]\n"
      "v_exp_f32 v214, v214\n"
      "v_exp_f32 v215, v215\n"
      "v_pk_add_f32 v[208:209], v[208:209], 1.0 op_sel_hi:[1,0]\n"
      "v_pk_add_f32 v[210:211], v[210:211], 1.0 op_sel_hi:[1,0]\n"
      "v_pk_add_f32 v[212:213], v[212:213], 1.0 op_sel_hi:[1,0]\n"
      "v_pk_add_f32 v[214:215], v[214:215], 1.0 op_sel_hi:[1,0]\n"
      "v_mfma_f32_16x16x32_bf16 v[76:79], v[224:227], v[152:155], v[76:79]\n"
      "v_rcp_f32 v208, v208\n"
      "v_rcp_f32 v209, v209\n"
      "v_mfma_f32_16x16x32_bf16 v[76:79], v[228:231], v[156:159], v[76:79]\n"
      "v_rcp_f32 v210, v210\n"
      "v_rcp_f32 v211, v211\n"
      "v_mfma_f32_16x16x32_bf16 v[92:95], v[232:235], v[152:155], v[92:95]\n"
      "v_rcp_f32 v212, v212\n"
      "v_rcp_f32 v213, v213\n"
      "v_mfma_f32_16x16x32_bf16 v[92:95], v[236:239], v[156:159], v[92:95]\n"
      "v_rcp_f32 v214, v214\n"
      "v_rcp_f32 v215, v215\n"
      "v_pk_mul_f32 v[208:209], v[208:209], v[40:41]\n"
      "v_pk_mul_f32 v[210:211], v[210:211], v[42:43]\n"
      "v_pk_mul_f32 v[212:213], v[212:213], v[56:57]\n"
      "v_pk_mul_f32 v[214:215], v[214:215], v[58:59]\n"
      "v_pk_mul_f32 v[208:209], v[208:209], v[72:73]\n"
      "v_pk_mul_f32 v[210:211], v[210:211], v[74:75]\n"
      "v_pk_mul_f32 v[212:213], v[212:213], v[88:89]\n"
      "v_pk_mul_f32 v[214:215], v[214:215], v[90:91]\n"
      "v_cvt_pk_bf16_f32 v200, v208, v209\n"
      "v_cvt_pk_bf16_f32 v201, v210, v211\n"
      "v_cvt_pk_bf16_f32 v202, v212, v213\n"
      "v_cvt_pk_bf16_f32 v203, v214, v215\n"
      "v_pk_mul_f32 v[208:209], v[44:45], s[74:75] op_sel_hi:[1,0]\n"
      "v_pk_mul_f32 v[210:211], v[46:47], s[74:75] op_sel_hi:[1,0]\n"
      "v_pk_mul_f32 v[212:213], v[60:61], s[74:75] op_sel_hi:[1,0]\n"
      "v_pk_mul_f32 v[214:215], v[62:63], s[74:75] op_sel_hi:[1,0]\n"
      "v_exp_f32 v208, v208\n"
      "v_exp_f32 v209, v209\n"
      "v_exp_f32 v210, v210\n"
      "v_exp_f32 v211, v211\n"
      "v_exp_f32 v212, v212\n"
      "v_exp_f32 v213, v213\n"
      "v_exp_f32 v214, v214\n"
      "v_exp_f32 v215, v215\n"
      "v_pk_add_f32 v[208:209], v[208:209], 1.0 op_sel_hi:[1,0]\n"
      "v_pk_add_f32 v[210:211], v[210:211], 1.0 op_sel_hi:[1,0]\n"
      "v_pk_add_f32 v[212:213], v[212:213], 1.0 op_sel_hi:[1,0]\n"
      "v_pk_add_f32 v[214:215], v[214:215], 1.0 op_sel_hi:[1,0]\n"
      "v_rcp_f32 v208, v208\n"
      "v_rcp_f32 v209, v209\n"
      "v_rcp_f32 v210, v210\n"
      "v_rcp_f32 v211, v211\n"
      "v_rcp_f32 v212, v212\n"
      "v_rcp_f32 v213, v213\n"
      "v_rcp_f32 v214, v214\n"
      "v_rcp_f32 v215, v215\n"
      "v_pk_mul_f32 v[208:209], v[208:209], v[44:45]\n"
      "v_pk_mul_f32 v[210:211], v[210:211], v[46:47]\n"
      "v_pk_mul_f32 v[212:213], v[212:213], v[60:61]\n"
      "v_pk_mul_f32 v[214:215], v[214:215], v[62:63]\n"
      "v_pk_mul_f32 v[208:209], v[208:209], v[76:77]\n"
      "v_pk_mul_f32 v[210:211], v[210:211], v[78:79]\n"
      "v_pk_mul_f32 v[212:213], v[212:213], v[92:93]\n"
      "v_pk_mul_f32 v[214:215], v[214:215], v[94:95]\n"
      "v_cvt_pk_bf16_f32 v204, v208, v209\n"
      "v_cvt_pk_bf16_f32 v205, v210, v211\n"
      "v_cvt_pk_bf16_f32 v206, v212, v213\n"
      "v_cvt_pk_bf16_f32 v207, v214, v215\n"
      "buffer_store_dwordx4 v[192:195], v244, s[20:23], 0 offen sc1\n"
      "buffer_store_dwordx4 v[196:199], v244, s[20:23], s76 offen sc1\n"
      "buffer_store_dwordx4 v[200:203], v244, s[20:23], s77 offen sc1\n"
      "buffer_store_dwordx4 v[204:207], v244, s[20:23], s78 offen sc1\n"
      "s_add_u32 m0, s47, 16384\n"
      "s_nop 0\n"
      "buffer_load_dwordx4 v2, s[16:19], s40 offen offset:0 lds\n"
      "buffer_load_dwordx4 v3, s[16:19], s40 offen offset:1024 lds\n"
      "buffer_load_dwordx4 v4, s[16:19], s40 offen offset:2048 lds\n"
      "buffer_load_dwordx4 v5, s[16:19], s40 offen offset:3072 lds\n"
      "s_add_u32 m0, s47, 32768\n"
      "s_nop 0\n"
      "buffer_load_dwordx4 v2, s[16:19], s41 offen offset:0 lds\n"
      "buffer_load_dwordx4 v3, s[16:19], s41 offen offset:1024 lds\n"
      "buffer_load_dwordx4 v4, s[16:19], s41 offen offset:2048 lds\n"
      "buffer_load_dwordx4 v5, s[16:19], s41 offen offset:3072 lds\n"
      "s_waitcnt vmcnt(8)\n"
      "s_mov_b64 exec, 0xffff\n"
      "buffer_store_dwordx2 v[30:31], v245, s[20:23], 0 offen sc1\n"
      "s_mov_b64 exec, -1\n"
      "ds_write_b32 v28, v29\n"
      "s_setprio 2\n"
      ".Lcons_w2_%=:\n"
      "s_add_u32 m0, s47, 49152\n"
      "s_nop 0\n"
      "buffer_load_dwordx4 v2, s[16:19], s42 offen offset:0 lds\n"
      "buffer_load_dwordx4 v3, s[16:19], s42 offen offset:1024 lds\n"
      "buffer_load_dwordx4 v4, s[16:19], s42 offen offset:2048 lds\n"
      "buffer_load_dwordx4 v5, s[16:19], s42 offen offset:3072 lds\n"
      "s_add_u32 m0, s47, 0\n"
      "s_nop 0\n"
      "buffer_load_dwordx4 v2, s[16:19], s43 offen offset:0 lds\n"
      "buffer_load_dwordx4 v3, s[16:19], s43 offen offset:1024 lds\n"
      "buffer_load_dwordx4 v4, s[16:19], s43 offen offset:2048 lds\n"
      "buffer_load_dwordx4 v5, s[16:19], s43 offen offset:3072 lds\n"
      "s_waitcnt vmcnt(12)\n"
      "ds_read_b64_tr_b16 v[96:97], v6 offset:16384\n"
      "ds_read_b64_tr_b16 v[98:99], v6 offset:18432\n"
      "ds_read_b64_tr_b16 v[100:101], v7 offset:16384\n"
      "ds_read_b64_tr_b16 v[102:103], v7 offset:18432\n"
      "ds_read_b64_tr_b16 v[104:105], v8 offset:16384\n"
      "ds_read_b64_tr_b16 v[106:107], v8 offset:18432\n"
      "ds_read_b64_tr_b16 v[108:109], v9 offset:16384\n"
      "ds_read_b64_tr_b16 v[110:111], v9 offset:18432\n"
      "s_waitcnt lgkmcnt(0)\n"
      "s_add_u32 m0, s47, 16384\n"
      "s_nop 0\n"
      "buffer_load_dwordx4 v2, s[16:19], s44 offen offset:0 lds\n"
      "buffer_load_dwordx4 v3, s[16:19], s44 offen offset:1024 lds\n"
      "buffer_load_dwordx4 v4, s[16:19], s44 offen offset:2048 lds\n"
      "buffer_load_dwordx4 v5, s[16:19], s44 offen offset:3072 lds\n"
      "s_waitcnt vmcnt(12)\n"
      "ds_read_b64_tr_b16 v[112:113], v6 offset:32768\n"
      "ds_read_b64_tr_b16 v[114:115], v6 offset:34816\n"
      "ds_read_b64_tr_b16 v[116:117], v7 offset:32768\n"
      "ds_read_b64_tr_b16 v[118:119], v7 offset:34816\n"
      "ds_read_b64_tr_b16 v[120:121], v8 offset:32768\n"
      "ds_read_b64_tr_b16 v[122:123], v8 offset:34816\n"
      "ds_read_b64_tr_b16 v[124:125], v9 offset:32768\n"
      "ds_read_b64_tr_b16 v[126:127], v9 offset:34816\n"
      "s_waitcnt lgkmcnt(0)\n"
      "s_add_u32 m0, s47, 32768\n"
      "s_nop 0\n"
      "buffer_load_dwordx4 v2, s[16:19], s45 offen offset:0 lds\n"
      "buffer_load_dwordx4 v3, s[16:19], s45 offen offset:1024 lds\n"
      "buffer_load_dwordx4 v4, s[16:19], s45 offen offset:2048 lds\n"
      "buffer_load_dwordx4 v5, s[16:19], s45 offen offset:3072 lds\n"
      "s_waitcnt vmcnt(12)\n"
      "ds_read_b64_tr_b16 v[128:129], v6 offset:49152\n"
      "ds_read_b64_tr_b16 v[130:131], v6 offset:51200\n"
      "ds_read_b64_tr_b16 v[132:133], v7 offset:49152\n"
      "ds_read_b64_tr_b16 v[134:135], v7 offset:51200\n"
      "ds_read_b64_tr_b16 v[136:137], v8 offset:49152\n"
      "ds_read_b64_tr_b16 v[138:139], v8 offset:51200\n"
      "ds_read_b64_tr_b16 v[140:141], v9 offset:49152\n"
      "ds_read_b64_tr_b16 v[142:143], v9 offset:51200\n"
      "s_waitcnt lgkmcnt(0)\n"
      "s_add_u32 m0, s47, 49152\n"
      "s_nop 0\n"
      "buffer_load_dwordx4 v2, s[16:19], s46 offen offset:0 lds\n"
      "buffer_load_dwordx4 v3, s[16:19], s46 offen offset:1024 lds\n"
      "buffer_load_dwordx4 v4, s[16:19], s46 offen offset:2048 lds\n"
      "buffer_load_dwordx4 v5, s[16:19], s46 offen offset:3072 lds\n"
      "v_mov_b64 v[236:237], 0\n"
      "s_mov_b64 exec, 0x7f\n"
      "buffer_atomic_add_x2 v[236:237], v18, s[20:23], 0 offen sc0 sc1\n"
      "s_mov_b64 exec, -1\n"
      "s_waitcnt vmcnt(13)\n"
      "ds_read_b64_tr_b16 v[144:145], v6 offset:0\n"
      "ds_read_b64_tr_b16 v[146:147], v6 offset:2048\n"
      "ds_read_b64_tr_b16 v[148:149], v7 offset:0\n"
      "ds_read_b64_tr_b16 v[150:151], v7 offset:2048\n"
      "ds_read_b64_tr_b16 v[152:153], v8 offset:0\n"
      "ds_read_b64_tr_b16 v[154:155], v8 offset:2048\n"
      "ds_read_b64_tr_b16 v[156:157], v9 offset:0\n"
      "ds_read_b64_tr_b16 v[158:159], v9 offset:2048\n"
      "s_waitcnt vmcnt(9)\n"
      "ds_read_b64_tr_b16 v[160:161], v6 offset:16384\n"
      "ds_read_b64_tr_b16 v[162:163], v6 offset:18432\n"
      "ds_read_b64_tr_b16 v[164:165], v7 offset:16384\n"
      "ds_read_b64_tr_b16 v[166:167], v7 offset:18432\n"
      "ds_read_b64_tr_b16 v[168:169], v8 offset:16384\n"
      "ds_read_b64_tr_b16 v[170:171], v8 offset:18432\n"
      "ds_read_b64_tr_b16 v[172:173], v9 offset:16384\n"
      "ds_read_b64_tr_b16 v[174:175], v9 offset:18432\n"
      "s_waitcnt vmcnt(5)\n"
      "ds_read_b64_tr_b16 v[176:177], v6 offset:32768\n"
      "ds_read_b64_tr_b16 v[178:179], v6 offset:34816\n"
      "ds_read_b64_tr_b16 v[180:181], v7 offset:32768\n"
      "ds_read_b64_tr_b16 v[182:183], v7 offset:34816\n"
      "ds_read_b64_tr_b16 v[184:185], v8 offset:32768\n"
      "ds_read_b64_tr_b16 v[186:187], v8 offset:34816\n"
      "ds_read_b64_tr_b16 v[188:189], v9 offset:32768\n"
      "ds_read_b64_tr_b16 v[190:191], v9 offset:34816\n"
      "s_waitcnt vmcnt(1)\n"
      "ds_read_b64_tr_b16 v[192:193], v6 offset:49152\n"
      "ds_read_b64_tr_b16 v[194:195], v6 offset:51200\n"
      "ds_read_b64_tr_b16 v[196:197], v7 offset:49152\n"
      "ds_read_b64_tr_b16 v[198:199], v7 offset:51200\n"
      "ds_read_b64_tr_b16 v[200:201], v8 offset:49152\n"
      "ds_read_b64_tr_b16 v[202:203], v8 offset:51200\n"
      "ds_read_b64_tr_b16 v[204:205], v9 offset:49152\n"
      "ds_read_b64_tr_b16 v[206:207], v9 offset:51200\n"
      ".Lp2_%=:\n"
      "s_setprio 2\n"
      "s_mov_b32 s73, 65536\n"
      ".Lflag_spin_%=:\n"
      "s_waitcnt vmcnt(0)\n"
      "v_cmp_ne_u64 vcc, s[36:37], v[236:237]\n"
      "s_and_b32 s55, vcc_lo, 0x7f\n"
      "s_cbranch_scc1 .Lflag_retry_%=\n"
      ".Lflag_ok_%=:\n"
      "buffer_load_dwordx4 v[208:211], v19, s[20:23], s48 offen sc1\n"
      "buffer_load_dwordx4 v[212:215], v19, s[20:23], s49 offen sc1\n"
      "buffer_load_dwordx4 v[216:219], v19, s[20:23], s50 offen sc1\n"
      "buffer_load_dwordx4 v[220:223], v19, s[20:23], s51 offen sc1\n"
      "buffer_load_dwordx4 v[224:227], v19, s[20:23], s52 offen sc1\n"
      "buffer_load_dwordx4 v[228:231], v19, s[20:23], s53 offen sc1\n"
      "s_and_b32 s56, s36, 0xff\n"
      "v_mov_b32 v240, s56\n"
      "s_sub_u32 s64, s36, 2135587861\n"
      "s_subb_u32 s65, s37, 2654435769\n"
      "s_lshr_b32 s64, s64, 8\n"
      "s_lshr_b32 s65, s65, 8\n"
      "s_add_u32 s55, s56, s64\n"
      "s_sub_u32 s55, s55, s65\n"
      "s_and_b32 s55, s55, 0xff\n"
      "v_mov_b32 v241, s55\n"
      "s_lshr_b32 s64, s64, 8\n"
      "s_lshr_b32 s65, s65, 8\n"
      "s_add_u32 s55, s56, s64\n"
      "s_sub_u32 s55, s55, s65\n"
      "s_and_b32 s55, s55, 0xff\n"
      "v_mov_b32 v242, s55\n"
      "s_lshr_b32 s64, s64, 8\n"
      "s_lshr_b32 s65, s65, 8\n"
      "s_add_u32 s55, s56, s64\n"
      "s_sub_u32 s55, s55, s65\n"
      "s_and_b32 s55, s55, 0xff\n"
      "v_mov_b32 v243, s55\n"
      "s_mov_b32 s57, 0xffffff00\n"
      "s_waitcnt lgkmcnt(0)\n"
      "s_waitcnt vmcnt(5)\n"
      "v_mfma_f32_16x16x32_bf16 v[32:35], v[96:99], v[208:211], 0\n"
      "v_mfma_f32_16x16x32_bf16 v[36:39], v[100:103], v[208:211], 0\n"
      "v_mfma_f32_16x16x32_bf16 v[40:43], v[104:107], v[208:211], 0\n"
      "v_mfma_f32_16x16x32_bf16 v[44:47], v[108:111], v[208:211], 0\n"
      "buffer_load_dwordx4 v[208:211], v19, s[20:23], s54 offen sc1\n"
      "s_waitcnt vmcnt(5)\n"
      "v_mfma_f32_16x16x32_bf16 v[32:35], v[112:115], v[212:215], v[32:35]\n"
      "v_mfma_f32_16x16x32_bf16 v[36:39], v[116:119], v[212:215], v[36:39]\n"
      "v_mfma_f32_16x16x32_bf16 v[40:43], v[120:123], v[212:215], v[40:43]\n"
      "v_mfma_f32_16x16x32_bf16 v[44:47], v[124:127], v[212:215], v[44:47]\n"
      "buffer_load_dwordx4 v[212:215], v20, s[20:23], s48 offen sc1\n"
      "s_waitcnt vmcnt(5)\n"
      "v_mfma_f32_16x16x32_bf16 v[32:35], v[128:131], v[216:219], v[32:35]\n"
      "v_mfma_f32_16x16x32_bf16 v[36:39], v[132:135], v[216:219], v[36:39]\n"
      "v_mfma_f32_16x16x32_bf16 v[40:43], v[136:139], v[216:219], v[40:43]\n"
      "v_mfma_f32_16x16x32_bf16 v[44:47], v[140:143], v[216:219], v[44:47]\n"
      "buffer_load_dwordx4 v[216:219], v20, s[20:23], s49 offen sc1\n"
      "s_waitcnt vmcnt(5)\n"
      "v_mfma_f32_16x16x32_bf16 v[32:35], v[144:147], v[220:223], v[32:35]\n"
      "v_mfma_f32_16x16x32_bf16 v[36:39], v[148:151], v[220:223], v[36:39]\n"
      "v_mfma_f32_16x16x32_bf16 v[40:43], v[152:155], v[220:223], v[40:43]\n"
      "v_mfma_f32_16x16x32_bf16 v[44:47], v[156:159], v[220:223], v[44:47]\n"
      "buffer_load_dwordx4 v[220:223], v20, s[20:23], s50 offen sc1\n"
      "s_waitcnt vmcnt(5)\n"
      "v_mfma_f32_16x16x32_bf16 v[32:35], v[160:163], v[224:227], v[32:35]\n"
      "v_mfma_f32_16x16x32_bf16 v[36:39], v[164:167], v[224:227], v[36:39]\n"
      "v_mfma_f32_16x16x32_bf16 v[40:43], v[168:171], v[224:227], v[40:43]\n"
      "v_mfma_f32_16x16x32_bf16 v[44:47], v[172:175], v[224:227], v[44:47]\n"
      "buffer_load_dwordx4 v[224:227], v20, s[20:23], s51 offen sc1\n"
      "s_waitcnt vmcnt(5)\n"
      "v_mfma_f32_16x16x32_bf16 v[32:35], v[176:179], v[228:231], v[32:35]\n"
      "v_mfma_f32_16x16x32_bf16 v[36:39], v[180:183], v[228:231], v[36:39]\n"
      "v_mfma_f32_16x16x32_bf16 v[40:43], v[184:187], v[228:231], v[40:43]\n"
      "v_mfma_f32_16x16x32_bf16 v[44:47], v[188:191], v[228:231], v[44:47]\n"
      "buffer_load_dwordx4 v[228:231], v20, s[20:23], s52 offen sc1\n"
      "s_waitcnt vmcnt(5)\n"
      "v_mfma_f32_16x16x32_bf16 v[32:35], v[192:195], v[208:211], v[32:35]\n"
      "v_mfma_f32_16x16x32_bf16 v[36:39], v[196:199], v[208:211], v[36:39]\n"
      "v_mfma_f32_16x16x32_bf16 v[40:43], v[200:203], v[208:211], v[40:43]\n"
      "v_mfma_f32_16x16x32_bf16 v[44:47], v[204:207], v[208:211], v[44:47]\n"
      "buffer_load_dwordx4 v[208:211], v20, s[20:23], s53 offen sc1\n"
      "s_waitcnt vmcnt(5)\n"
      "v_mfma_f32_16x16x32_bf16 v[48:51], v[96:99], v[212:215], 0\n"
      "v_mfma_f32_16x16x32_bf16 v[52:55], v[100:103], v[212:215], 0\n"
      "v_mfma_f32_16x16x32_bf16 v[56:59], v[104:107], v[212:215], 0\n"
      "v_mfma_f32_16x16x32_bf16 v[60:63], v[108:111], v[212:215], 0\n"
      "buffer_load_dwordx4 v[212:215], v20, s[20:23], s54 offen sc1\n"
      "ds_write_b128 v10, v[32:35] offset:0\n"
      "s_waitcnt vmcnt(5)\n"
      "v_mfma_f32_16x16x32_bf16 v[48:51], v[112:115], v[216:219], v[48:51]\n"
      "v_mfma_f32_16x16x32_bf16 v[52:55], v[116:119], v[216:219], v[52:55]\n"
      "v_mfma_f32_16x16x32_bf16 v[56:59], v[120:123], v[216:219], v[56:59]\n"
      "v_mfma_f32_16x16x32_bf16 v[60:63], v[124:127], v[216:219], v[60:63]\n"
      "buffer_load_dwordx4 v[216:219], v21, s[20:23], s48 offen sc1\n"
      "ds_write_b128 v11, v[36:39] offset:1024\n"
      "s_waitcnt vmcnt(5)\n"
      "v_mfma_f32_16x16x32_bf16 v[48:51], v[128:131], v[220:223], v[48:51]\n"
      "v_mfma_f32_16x16x32_bf16 v[52:55], v[132:135], v[220:223], v[52:55]\n"
      "v_mfma_f32_16x16x32_bf16 v[56:59], v[136:139], v[220:223], v[56:59]\n"
      "v_mfma_f32_16x16x32_bf16 v[60:63], v[140:143], v[220:223], v[60:63]\n"
      "buffer_load_dwordx4 v[220:223], v21, s[20:23], s49 offen sc1\n"
      "ds_write_b128 v12, v[40:43] offset:2048\n"
      "s_waitcnt vmcnt(5)\n"
      "v_mfma_f32_16x16x32_bf16 v[48:51], v[144:147], v[224:227], v[48:51]\n"
      "v_mfma_f32_16x16x32_bf16 v[52:55], v[148:151], v[224:227], v[52:55]\n"
      "v_mfma_f32_16x16x32_bf16 v[56:59], v[152:155], v[224:227], v[56:59]\n"
      "v_mfma_f32_16x16x32_bf16 v[60:63], v[156:159], v[224:227], v[60:63]\n"
      "buffer_load_dwordx4 v[224:227], v21, s[20:23], s50 offen sc1\n"
      "ds_write_b128 v13, v[44:47] offset:3072\n"
      "s_waitcnt vmcnt(5)\n"
      "v_mfma_f32_16x16x32_bf16 v[48:51], v[160:163], v[228:231], v[48:51]\n"
      "v_mfma_f32_16x16x32_bf16 v[52:55], v[164:167], v[228:231], v[52:55]\n"
      "v_mfma_f32_16x16x32_bf16 v[56:59], v[168:171], v[228:231], v[56:59]\n"
      "v_mfma_f32_16x16x32_bf16 v[60:63], v[172:175], v[228:231], v[60:63]\n"
      "buffer_load_dwordx4 v[228:231], v21, s[20:23], s51 offen sc1\n"
      "s_waitcnt vmcnt(5)\n"
      "v_mfma_f32_16x16x32_bf16 v[48:51], v[176:179], v[208:211], v[48:51]\n"
      "v_mfma_f32_16x16x32_bf16 v[52:55], v[180:183], v[208:211], v[52:55]\n"
      "v_mfma_f32_16x16x32_bf16 v[56:59], v[184:187], v[208:211], v[56:59]\n"
      "v_mfma_f32_16x16x32_bf16 v[60:63], v[188:191], v[208:211], v[60:63]\n"
      "buffer_load_dwordx4 v[208:211], v21, s[20:23], s52 offen sc1\n"
      "s_waitcnt vmcnt(5)\n"
      "v_mfma_f32_16x16x32_bf16 v[48:51], v[192:195], v[212:215], v[48:51]\n"
      "v_mfma_f32_16x16x32_bf16 v[52:55], v[196:199], v[212:215], v[52:55]\n"
      "v_mfma_f32_16x16x32_bf16 v[56:59], v[200:203], v[212:215], v[56:59]\n"
      "v_mfma_f32_16x16x32_bf16 v[60:63], v[204:207], v[212:215], v[60:63]\n"
      "buffer_load_dwordx4 v[212:215], v21, s[20:23], s53 offen sc1\n"
      "s_setprio 1\n"
      "s_waitcnt vmcnt(5)\n"
      "v_mfma_f32_16x16x32_bf16 v[32:35], v[96:99], v[216:219], 0\n"
      "v_mfma_f32_16x16x32_bf16 v[36:39], v[100:103], v[216:219], 0\n"
      "v_mfma_f32_16x16x32_bf16 v[40:43], v[104:107], v[216:219], 0\n"
      "v_mfma_f32_16x16x32_bf16 v[44:47], v[108:111], v[216:219], 0\n"
      "buffer_load_dwordx4 v[216:219], v21, s[20:23], s54 offen sc1\n"
      "ds_write_b128 v10, v[48:51] offset:16384\n"
      "ds_write_b128 v11, v[52:55] offset:17408\n"
      "s_waitcnt vmcnt(5)\n"
      "v_mfma_f32_16x16x32_bf16 v[32:35], v[112:115], v[220:223], v[32:35]\n"
      "v_mfma_f32_16x16x32_bf16 v[36:39], v[116:119], v[220:223], v[36:39]\n"
      "v_mfma_f32_16x16x32_bf16 v[40:43], v[120:123], v[220:223], v[40:43]\n"
      "v_mfma_f32_16x16x32_bf16 v[44:47], v[124:127], v[220:223], v[44:47]\n"
      "buffer_load_dwordx4 v[220:223], v22, s[20:23], s48 offen sc1\n"
      "ds_write_b128 v12, v[56:59] offset:18432\n"
      "ds_write_b128 v13, v[60:63] offset:19456\n"
      "ds_write_b32 v27, v29\n"
      "ds_read_b32 v238, v26\n"
      "s_waitcnt vmcnt(5)\n"
      "v_mfma_f32_16x16x32_bf16 v[32:35], v[128:131], v[224:227], v[32:35]\n"
      "v_mfma_f32_16x16x32_bf16 v[36:39], v[132:135], v[224:227], v[36:39]\n"
      "v_mfma_f32_16x16x32_bf16 v[40:43], v[136:139], v[224:227], v[40:43]\n"
      "v_mfma_f32_16x16x32_bf16 v[44:47], v[140:143], v[224:227], v[44:47]\n"
      "buffer_load_dwordx4 v[224:227], v22, s[20:23], s49 offen sc1\n"
      "s_waitcnt lgkmcnt(0)\n"
      "v_cmp_eq_u32 vcc, 0, v238\n"
      "s_or_b64 vcc, vcc, s[62:63]\n"
      "s_cbranch_scc1 .Lret16_%=\n"
      "s_call_b64 s[60:61], .Lsend_%=\n"
      ".Lret16_%=:\n"
      "s_waitcnt vmcnt(5)\n"
      "v_mfma_f32_16x16x32_bf16 v[32:35], v[144:147], v[228:231], v[32:35]\n"
      "v_mfma_f32_16x16x32_bf16 v[36:39], v[148:151], v[228:231], v[36:39]\n"
      "v_mfma_f32_16x16x32_bf16 v[40:43], v[152:155], v[228:231], v[40:43]\n"
      "v_mfma_f32_16x16x32_bf16 v[44:47], v[156:159], v[228:231], v[44:47]\n"
      "buffer_load_dwordx4 v[228:231], v22, s[20:23], s50 offen sc1\n"
      "s_waitcnt vmcnt(5)\n"
      "v_mfma_f32_16x16x32_bf16 v[32:35], v[160:163], v[208:211], v[32:35]\n"
      "v_mfma_f32_16x16x32_bf16 v[36:39], v[164:167], v[208:211], v[36:39]\n"
      "v_mfma_f32_16x16x32_bf16 v[40:43], v[168:171], v[208:211], v[40:43]\n"
      "v_mfma_f32_16x16x32_bf16 v[44:47], v[172:175], v[208:211], v[44:47]\n"
      "buffer_load_dwordx4 v[208:211], v22, s[20:23], s51 offen sc1\n"
      "ds_read_b32 v238, v26\n"
      "s_waitcnt vmcnt(5)\n"
      "v_mfma_f32_16x16x32_bf16 v[32:35], v[176:179], v[212:215], v[32:35]\n"
      "v_mfma_f32_16x16x32_bf16 v[36:39], v[180:183], v[212:215], v[36:39]\n"
      "v_mfma_f32_16x16x32_bf16 v[40:43], v[184:187], v[212:215], v[40:43]\n"
      "v_mfma_f32_16x16x32_bf16 v[44:47], v[188:191], v[212:215], v[44:47]\n"
      "buffer_load_dwordx4 v[212:215], v22, s[20:23], s52 offen sc1\n"
      "s_waitcnt lgkmcnt(0)\n"
      "v_cmp_eq_u32 vcc, 0, v238\n"
      "s_or_b64 vcc, vcc, s[62:63]\n"
      "s_cbranch_scc1 .Lret19_%=\n"
      "s_call_b64 s[60:61], .Lsend_%=\n"
      ".Lret19_%=:\n"
      "s_waitcnt vmcnt(5)\n"
      "v_mfma_f32_16x16x32_bf16 v[32:35], v[192:195], v[216:219], v[32:35]\n"
      "v_mfma_f32_16x16x32_bf16 v[36:39], v[196:199], v[216:219], v[36:39]\n"
      "v_mfma_f32_16x16x32_bf16 v[40:43], v[200:203], v[216:219], v[40:43]\n"
      "v_mfma_f32_16x16x32_bf16 v[44:47], v[204:207], v[216:219], v[44:47]\n"
      "buffer_load_dwordx4 v[216:219], v22, s[20:23], s53 offen sc1\n"
      "s_setprio 0\n"
      "s_waitcnt vmcnt(5)\n"
      "v_mfma_f32_16x16x32_bf16 v[48:51], v[96:99], v[220:223], 0\n"
      "v_mfma_f32_16x16x32_bf16 v[52:55], v[100:103], v[220:223], 0\n"
      "v_mfma_f32_16x16x32_bf16 v[56:59], v[104:107], v[220:223], 0\n"
      "v_mfma_f32_16x16x32_bf16 v[60:63], v[108:111], v[220:223], 0\n"
      "buffer_load_dwordx4 v[220:223], v22, s[20:23], s54 offen sc1\n"
      "ds_write_b128 v10, v[32:35] offset:32768\n"
      "ds_read_b32 v238, v26\n"
      "s_waitcnt vmcnt(5)\n"
      "v_mfma_f32_16x16x32_bf16 v[48:51], v[112:115], v[224:227], v[48:51]\n"
      "v_mfma_f32_16x16x32_bf16 v[52:55], v[116:119], v[224:227], v[52:55]\n"
      "v_mfma_f32_16x16x32_bf16 v[56:59], v[120:123], v[224:227], v[56:59]\n"
      "v_mfma_f32_16x16x32_bf16 v[60:63], v[124:127], v[224:227], v[60:63]\n"
      "ds_write_b128 v11, v[36:39] offset:33792\n"
      "s_waitcnt lgkmcnt(1)\n"
      "v_cmp_eq_u32 vcc, 0, v238\n"
      "s_or_b64 vcc, vcc, s[62:63]\n"
      "s_cbranch_scc1 .Lret22_%=\n"
      "s_call_b64 s[60:61], .Lsend_%=\n"
      ".Lret22_%=:\n"
      "s_waitcnt vmcnt(4)\n"
      "v_mfma_f32_16x16x32_bf16 v[48:51], v[128:131], v[228:231], v[48:51]\n"
      "v_mfma_f32_16x16x32_bf16 v[52:55], v[132:135], v[228:231], v[52:55]\n"
      "v_mfma_f32_16x16x32_bf16 v[56:59], v[136:139], v[228:231], v[56:59]\n"
      "v_mfma_f32_16x16x32_bf16 v[60:63], v[140:143], v[228:231], v[60:63]\n"
      "ds_write_b128 v12, v[40:43] offset:34816\n"
      "s_waitcnt vmcnt(3)\n"
      "v_mfma_f32_16x16x32_bf16 v[48:51], v[144:147], v[208:211], v[48:51]\n"
      "v_mfma_f32_16x16x32_bf16 v[52:55], v[148:151], v[208:211], v[52:55]\n"
      "v_mfma_f32_16x16x32_bf16 v[56:59], v[152:155], v[208:211], v[56:59]\n"
      "v_mfma_f32_16x16x32_bf16 v[60:63], v[156:159], v[208:211], v[60:63]\n"
      "ds_write_b128 v13, v[44:47] offset:35840\n"
      "ds_read_b32 v238, v26\n"
      "s_waitcnt vmcnt(2)\n"
      "v_mfma_f32_16x16x32_bf16 v[48:51], v[160:163], v[212:215], v[48:51]\n"
      "v_mfma_f32_16x16x32_bf16 v[52:55], v[164:167], v[212:215], v[52:55]\n"
      "v_mfma_f32_16x16x32_bf16 v[56:59], v[168:171], v[212:215], v[56:59]\n"
      "v_mfma_f32_16x16x32_bf16 v[60:63], v[172:175], v[212:215], v[60:63]\n"
      "s_waitcnt lgkmcnt(0)\n"
      "v_cmp_eq_u32 vcc, 0, v238\n"
      "s_or_b64 vcc, vcc, s[62:63]\n"
      "s_cbranch_scc1 .Lret25_%=\n"
      "s_call_b64 s[60:61], .Lsend_%=\n"
      ".Lret25_%=:\n"
      "s_waitcnt vmcnt(1)\n"
      "v_mfma_f32_16x16x32_bf16 v[48:51], v[176:179], v[216:219], v[48:51]\n"
      "v_mfma_f32_16x16x32_bf16 v[52:55], v[180:183], v[216:219], v[52:55]\n"
      "v_mfma_f32_16x16x32_bf16 v[56:59], v[184:187], v[216:219], v[56:59]\n"
      "v_mfma_f32_16x16x32_bf16 v[60:63], v[188:191], v[216:219], v[60:63]\n"
      "s_waitcnt vmcnt(0)\n"
      "v_mfma_f32_16x16x32_bf16 v[48:51], v[192:195], v[220:223], v[48:51]\n"
      "v_mfma_f32_16x16x32_bf16 v[52:55], v[196:199], v[220:223], v[52:55]\n"
      "v_mfma_f32_16x16x32_bf16 v[56:59], v[200:203], v[220:223], v[56:59]\n"
      "v_mfma_f32_16x16x32_bf16 v[60:63], v[204:207], v[220:223], v[60:63]\n"
      "buffer_load_dwordx4 v[232:235], v24, s[20:23], 0 offen sc1\n"
      "s_nop 3\n"
      "ds_write_b128 v10, v[48:51] offset:49152\n"
      "ds_write_b128 v11, v[52:55] offset:50176\n"
      "ds_write_b128 v12, v[56:59] offset:51200\n"
      "ds_write_b128 v13, v[60:63] offset:52224\n"
      "s_cmp_lg_u64 s[62:63], 0\n"
      "s_cbranch_scc0 .Larr_wait_%=\n"
      ".Lret_end_%=:\n"
      "s_mov_b32 s73, 65536\n"
      "s_waitcnt lgkmcnt(0)\n"
      "s_barrier\n"
      "ds_read_b128 v[64:67], v16 offset:0\n"
      "ds_read_b128 v[68:71], v16 offset:4096\n"
      "ds_read_b128 v[72:75], v16 offset:8192\n"
      "ds_read_b128 v[76:79], v16 offset:12288\n"
      "ds_read_b128 v[80:83], v17 offset:0\n"
      "ds_read_b128 v[84:87], v17 offset:4096\n"
      "ds_read_b128 v[88:91], v17 offset:8192\n"
      "ds_read_b128 v[92:95], v17 offset:12288\n"
      "s_waitcnt lgkmcnt(6)\n"
      "v_pk_add_f32 v[64:65], v[64:65], v[68:69]\n"
      "v_pk_add_f32 v[66:67], v[66:67], v[70:71]\n"
      "s_waitcnt lgkmcnt(4)\n"
      "v_pk_add_f32 v[72:73], v[72:73], v[76:77]\n"
      "v_pk_add_f32 v[74:75], v[74:75], v[78:79]\n"
      "s_waitcnt lgkmcnt(2)\n"
      "v_pk_add_f32 v[80:81], v[80:81], v[84:85]\n"
      "v_pk_add_f32 v[82:83], v[82:83], v[86:87]\n"
      "s_waitcnt lgkmcnt(0)\n"
      "v_pk_add_f32 v[88:89], v[88:89], v[92:93]\n"
      "v_pk_add_f32 v[90:91], v[90:91], v[94:95]\n"
      "v_pk_add_f32 v[64:65], v[64:65], v[72:73]\n"
      "v_pk_add_f32 v[66:67], v[66:67], v[74:75]\n"
      "v_pk_add_f32 v[80:81], v[80:81], v[88:89]\n"
      "v_pk_add_f32 v[82:83], v[82:83], v[90:91]\n"
      "v_pk_add_f32 v[64:65], v[64:65], v[80:81]\n"
      "v_pk_add_f32 v[66:67], v[66:67], v[82:83]\n"
      ".Lx_spin_%=:\n"
      "s_waitcnt vmcnt(0)\n"
      "v_xor_b32 v68, v232, v240\n"
      "v_xor_b32 v69, v233, v241\n"
      "v_xor_b32 v70, v234, v242\n"
      "v_xor_b32 v71, v235, v243\n"
      "v_or3_b32 v72, v68, v69, v70\n"
      "v_pk_add_f32 v[68:69], v[64:65], v[68:69]\n"
      "v_or_b32 v72, v72, v71\n"
      "v_pk_add_f32 v[70:71], v[66:67], v[70:71]\n"
      "v_and_b32 v72, 0xff, v72\n"
      "v_cvt_pk_bf16_f32 v68, v68, v69\n"
      "v_cmp_ne_u32 vcc, 0, v72\n"
      "v_cvt_pk_bf16_f32 v69, v70, v71\n"
      "s_cbranch_vccnz .Lx_retry_%=\n"
      ".Lx_ok_%=:\n"
      "buffer_store_dwordx2 v[68:69], v25, s[24:27], 0 offen sc1\n"
      "s_endpgm\n"
      ".Lsend_%=:\n"
      "ds_read_b128 v[64:67], v14 offset:0\n"
      "ds_read_b128 v[68:71], v14 offset:4096\n"
      "ds_read_b128 v[72:75], v14 offset:8192\n"
      "ds_read_b128 v[76:79], v14 offset:12288\n"
      "ds_read_b128 v[80:83], v15 offset:0\n"
      "ds_read_b128 v[84:87], v15 offset:4096\n"
      "ds_read_b128 v[88:91], v15 offset:8192\n"
      "ds_read_b128 v[92:95], v15 offset:12288\n"
      "s_waitcnt lgkmcnt(6)\n"
      "v_pk_add_f32 v[64:65], v[64:65], v[68:69]\n"
      "v_pk_add_f32 v[66:67], v[66:67], v[70:71]\n"
      "s_waitcnt lgkmcnt(4)\n"
      "v_pk_add_f32 v[72:73], v[72:73], v[76:77]\n"
      "v_pk_add_f32 v[74:75], v[74:75], v[78:79]\n"
      "s_waitcnt lgkmcnt(2)\n"
      "v_pk_add_f32 v[80:81], v[80:81], v[84:85]\n"
      "v_pk_add_f32 v[82:83], v[82:83], v[86:87]\n"
      "s_waitcnt lgkmcnt(0)\n"
      "v_pk_add_f32 v[88:89], v[88:89], v[92:93]\n"
      "v_pk_add_f32 v[90:91], v[90:91], v[94:95]\n"
      "v_pk_add_f32 v[64:65], v[64:65], v[72:73]\n"
      "v_pk_add_f32 v[66:67], v[66:67], v[74:75]\n"
      "v_pk_add_f32 v[80:81], v[80:81], v[88:89]\n"
      "v_pk_add_f32 v[82:83], v[82:83], v[90:91]\n"
      "v_pk_add_f32 v[64:65], v[64:65], v[80:81]\n"
      "v_pk_add_f32 v[66:67], v[66:67], v[82:83]\n"
      "v_add_u32 v64, 0x80, v64\n"
      "v_add_u32 v65, 0x80, v65\n"
      "v_add_u32 v66, 0x80, v66\n"
      "v_add_u32 v67, 0x80, v67\n"
      "v_and_or_b32 v64, v64, s57, v240\n"
      "v_and_or_b32 v65, v65, s57, v241\n"
      "v_and_or_b32 v66, v66, s57, v242\n"
      "v_and_or_b32 v67, v67, s57, v243\n"
      "buffer_store_dwordx4 v[64:67], v23, s[20:23], 0 offen sc1\n"
      "s_mov_b64 s[62:63], -1\n"
      "s_setpc_b64 s[60:61]\n"
      ".Lx_retry_%=:\n"
      "s_sub_u32 s73, s73, 1\n"
      "s_cbranch_scc1 .Lx_ok_%=\n"
      "buffer_load_dwordx4 v[232:235], v24, s[20:23], 0 offen sc1\n"
      "s_branch .Lx_spin_%=\n"
      ".Lflag_retry_%=:\n"
      "s_sub_u32 s73, s73, 1\n"
      "s_cbranch_scc1 .Lflag_ok_%=\n"
      "s_sleep 2\n"
      "v_mov_b64 v[236:237], 0\n"
      "s_mov_b64 exec, 0x7f\n"
      "buffer_atomic_add_x2 v[236:237], v18, s[20:23], 0 offen sc0 sc1\n"
      "s_mov_b64 exec, -1\n"
      "s_branch .Lflag_spin_%=\n"
      ".Larr_wait_%=:\n"
      "s_mov_b32 s73, 4194304\n"
      ".Larr_spin_%=:\n"
      "ds_read_b32 v238, v26\n"
      "s_sub_u32 s73, s73, 1\n"
      "s_cbranch_scc1 .Larr_ok_%=\n"
      "s_waitcnt lgkmcnt(0)\n"
      "v_cmp_eq_u32 vcc, 0, v238\n"
      "s_cbranch_vccnz .Larr_spin_%=\n"
      ".Larr_ok_%=:\n"
      "s_call_b64 s[60:61], .Lsend_%=\n"
      ".Larr_ret_%=:\n"
      "s_branch .Lret_end_%=\n"
      ".Lhelp_%=:\n"
      "s_waitcnt lgkmcnt(0)\n"
      "s_barrier\n"
      "v_mov_b32 v30, 147456\n"
      "ds_read_b64 v[30:31], v30\n"
      "s_waitcnt lgkmcnt(0)\n"
      "v_readfirstlane_b32 s36, v30\n"
      "v_readfirstlane_b32 s37, v31\n"
      "s_cmp_lg_u32 s28, 1\n"
      "s_cbranch_scc1 .Lhelp_w2_%=\n"
      "s_cmp_lg_u32 %1, 255\n"
      "s_cbranch_scc1 .Lhelp_w2_%=\n"
      "v_lshlrev_b32 v246, 7, v1\n"
      "v_add_u32 v246, 6291456, v246\n"
      "v_add_u32 v247, 8192, v246\n"
      "v_add_u32 v248, 16384, v246\n"
      "v_add_u32 v249, 24576, v246\n"
      "s_mov_b32 s73, 65536\n"
      ".Lbump_spin_%=:\n"
      "buffer_load_dwordx2 v[32:33], v246, s[20:23], 0 offen sc1\n"
      "buffer_load_dwordx2 v[34:35], v247, s[20:23], 0 offen sc1\n"
      "buffer_load_dwordx2 v[36:37], v248, s[20:23], 0 offen sc1\n"
      "buffer_load_dwordx2 v[38:39], v249, s[20:23], 0 offen sc1\n"
      "s_waitcnt vmcnt(0)\n"
      "v_cmp_ne_u64 s[64:65], s[36:37], v[32:33]\n"
      "v_cmp_ne_u64 s[66:67], s[36:37], v[34:35]\n"
      "v_cmp_ne_u64 s[68:69], s[36:37], v[36:37]\n"
      "v_cmp_ne_u64 vcc, s[36:37], v[38:39]\n"
      "s_or_b64 s[64:65], s[64:65], s[66:67]\n"
      "s_or_b64 s[64:65], s[64:65], s[68:69]\n"
      "s_or_b64 s[64:65], s[64:65], vcc\n"
      "s_cbranch_scc0 .Lbump_%=\n"
      "s_sleep 8\n"
      "s_sub_u32 s73, s73, 1\n"
      "s_cbranch_scc0 .Lbump_spin_%=\n"
      ".Lbump_%=:\n"
      "s_sub_u32 s55, s36, 2135587860\n"
      "s_subb_u32 s56, s37, 2654435769\n"
      "v_mov_b32 v32, s55\n"
      "v_mov_b32 v33, s56\n"
      "v_mov_b32 v34, 6324224\n"
      "s_mov_b64 exec, 1\n"
      "buffer_store_dwordx2 v[32:33], v34, s[20:23], 0 offen sc1\n"
      "s_mov_b64 exec, -1\n"
      ".Lhelp_w2_%=:\n"
      "s_add_u32 m0, s47, 16384\n"
      "s_nop 0\n"
      "buffer_load_dwordx4 v2, s[16:19], s40 offen offset:0 lds\n"
      "buffer_load_dwordx4 v3, s[16:19], s40 offen offset:1024 lds\n"
      "buffer_load_dwordx4 v4, s[16:19], s40 offen offset:2048 lds\n"
      "buffer_load_dwordx4 v5, s[16:19], s40 offen offset:3072 lds\n"
      "s_add_u32 m0, s47, 32768\n"
      "s_nop 0\n"
      "buffer_load_dwordx4 v2, s[16:19], s41 offen offset:0 lds\n"
      "buffer_load_dwordx4 v3, s[16:19], s41 offen offset:1024 lds\n"
      "buffer_load_dwordx4 v4, s[16:19], s41 offen offset:2048 lds\n"
      "buffer_load_dwordx4 v5, s[16:19], s41 offen offset:3072 lds\n"
      "s_branch .Lcons_w2_%=\n"
      ".Lend_%=:\n"
      :
      : "s"(__builtin_amdgcn_kernarg_segment_ptr()), "s"(blockIdx.x), "v"(threadIdx.x),
        "s"((unsigned)(size_t)(__attribute__((address_space(3))) char*)smem)
      : "memory", "vcc", "scc",
        "s4", "s5", "s6", "s7", "s8", "s9", "s10", "s11", "s12", "s13", "s14", "s15", "s16", "s17",
        "s18", "s19", "s20", "s21", "s22", "s23", "s24", "s25", "s26", "s27", "s28", "s29", "s30", "s31",
        "s33", "s34", "s35", "s36", "s37", "s38", "s39", "s40", "s41", "s42", "s43", "s44", "s45", "s46",
        "s47", "s48", "s49", "s50", "s51", "s52", "s53", "s54", "s55", "s56", "s57", "s58", "s59", "s60",
        "s61", "s62", "s63", "s64", "s65", "s66", "s67", "s68", "s69", "s70", "s71", "s72", "s73", "s74",
        "s75", "s76", "s77", "s78", "v1", "v2", "v3", "v4", "v5", "v6", "v7", "v8", "v9", "v10",
        "v11", "v12", "v13", "v14", "v15", "v16", "v17", "v18", "v19", "v20", "v21", "v22", "v23", "v24",
        "v25", "v26", "v27", "v28", "v29", "v30", "v31", "v32", "v33", "v34", "v35", "v36", "v37", "v38",
        "v39", "v40", "v41", "v42", "v43", "v44", "v45", "v46", "v47", "v48", "v49", "v50", "v51", "v52",
        "v53", "v54", "v55", "v56", "v57", "v58", "v59", "v60", "v61", "v62", "v63", "v64", "v65", "v66",
        "v67", "v68", "v69", "v70", "v71", "v72", "v73", "v74", "v75", "v76", "v77", "v78", "v79", "v80",
        "v81", "v82", "v83", "v84", "v85", "v86", "v87", "v88", "v89", "v90", "v91", "v92", "v93", "v94",
        "v95", "v96", "v97", "v98", "v99", "v100", "v101", "v102", "v103", "v104", "v105", "v106", "v107", "v108",
        "v109", "v110", "v111", "v112", "v113", "v114", "v115", "v116", "v117", "v118", "v119", "v120", "v121", "v122",
        "v123", "v124", "v125", "v126", "v127", "v128", "v129", "v130", "v131", "v132", "v133", "v134", "v135", "v136",
        "v137", "v138", "v139", "v140", "v141", "v142", "v143", "v144", "v145", "v146", "v147", "v148", "v149", "v150",
        "v151", "v152", "v153", "v154", "v155", "v156", "v157", "v158", "v159", "v160", "v161", "v162", "v163", "v164",
        "v165", "v166", "v167", "v168", "v169", "v170", "v171", "v172", "v173", "v174", "v175", "v176", "v177", "v178",
        "v179", "v180", "v181", "v182", "v183", "v184", "v185", "v186", "v187", "v188", "v189", "v190", "v191", "v192",
        "v193", "v194", "v195", "v196", "v197", "v198", "v199", "v200", "v201", "v202", "v203", "v204", "v205", "v206",
        "v207", "v208", "v209", "v210", "v211", "v212", "v213", "v214", "v215", "v216", "v217", "v218", "v219", "v220",
        "v221", "v222", "v223", "v224", "v225", "v226", "v227", "v228", "v229", "v230", "v231", "v232", "v233", "v234",
        "v235", "v236", "v237", "v238", "v239", "v240", "v241", "v242", "v243", "v244", "v245", "v246", "v247", "v248",
        "v249", "v250", "v251", "v252");
}

extern "C" void kernel_launch(void* const* d_in, const int* in_sizes, int n_in, void* d_out, int out_size, void* d_ws,
                              size_t ws_size, hipStream_t stream) {
  constexpr unsigned kAct = 512 * 1024 * 2, kWt = 1024 * 3584 * 2, kFmt = 0x20000;
  swiglu_fused<<<256, 512, 0, stream>>>(d_in[0], kAct, kFmt, d_in[1], kWt, kFmt, d_in[2], kWt + 2048, kFmt, d_in[3], kWt, kFmt,
                                        d_ws, 6328320u, kFmt, d_out, kAct, kFmt);
}
```
